```python
import torch

def _softmax(x, axis=-1):
    return torch.softmax(x, dim=axis)
def _gelu(x):
    return torch.nn.functional.gelu(x, approximate="tanh")
def _silu(x):
    return torch.nn.functional.silu(x)
def _sigmoid(x):
    return torch.sigmoid(x)
def _layernorm(x, axis=-1, eps=1e-5):
    m = x.mean(dim=axis, keepdim=True)
    v = x.var(dim=axis, keepdim=True, unbiased=False)
    return (x - m) / torch.sqrt(v + eps)
def _scatter_add(tgt, idx, src, axis=0):
    return tgt.clone().index_add_(axis, idx, src)
def _segment_sum(src, seg, n):
    out = torch.zeros((n,) + src.shape[1:], dtype=src.dtype)
    return out.index_add_(0, seg, src)
def _pad(x, pw):
    flat = tuple(v for pair in reversed(pw) for v in pair)
    return torch.nn.functional.pad(x, flat)

kernel_name = 'recurrent_scan__linear_attn_block__mixer_s_mlp__prompt'

def setup_inputs():

    _w = (lambda a: (lambda t: t.to(torch.bfloat16) if t.is_floating_point() else t)(torch.as_tensor(a)))
    def _A_setup():
        B, S, D = 1, 2048, 512
        return {
            "x": _w((lambda *s: torch.randn(*s, dtype=torch.bfloat16) * 0.05)(B, S, D)),
            "wqkv": _w((lambda *s: torch.randn(*s, dtype=torch.bfloat16) * 0.05)(D, 3 * D)),
            "wo": _w((lambda *s: torch.randn(*s, dtype=torch.bfloat16) * 0.05)(D, D)),
        }
    _A_in = _A_setup()

    def _B_setup():
        B, S, D, F = 1, 2048, 512, 1792
        return {
            "x":  _w((lambda *s: torch.randn(*s, dtype=torch.bfloat16) * 0.05)(B, S, D)),
            "w1": _w((lambda *s: torch.randn(*s, dtype=torch.bfloat16) * 3.2)(D, D) * (D ** -0.5)),
            "w2": _w((lambda *s: torch.randn(*s, dtype=torch.bfloat16) * 3.2)(D, D) * (D ** -0.5)),
            "w3": _w((lambda *s: torch.randn(*s, dtype=torch.bfloat16) * 3.2)(D, F) * (D ** -0.5)),
            "w4": _w((lambda *s: torch.randn(*s, dtype=torch.bfloat16) * 1.6)(F, D) * (F ** -0.5)),
        }
    _B_in = _B_setup()

    return {
        "A_x": _A_in["x"],
        "A_wqkv": _A_in["wqkv"],
        "A_wo": _A_in["wo"],
        "B_x": _B_in["x"],
        "B_w1": _B_in["w1"],
        "B_w2": _B_in["w2"],
        "B_w3": _B_in["w3"],
        "B_w4": _B_in["w4"],
    }

def reference(A_x, A_wqkv, A_wo, B_x, B_w1, B_w2, B_w3, B_w4):
    def _A_ref(x, wqkv, wo):
        B, S, D = x.shape
        H = 32; Dh = D // H
        qkv = (lambda x, *d: x.permute(*d))((x @ wqkv).reshape(B, S, 3, H, Dh), 2, 0, 3, 1, 4)
        q, k, v = qkv[0], qkv[1], qkv[2]
        phi = lambda t: torch.where(t > 0, t + 1, torch.exp(t)) + 1e-6
        pq, pk = phi(q), phi(k)
        kv = torch.einsum("bhsd,bhse->bhde", pk, v)
        z = torch.einsum("bhsd->bhd", pk)
        num = torch.einsum("bhqd,bhde->bhqe", pq, kv)
        den = torch.einsum("bhqd,bhd->bhq", pq, z)[..., None] + 1e-6
        o = num / den
        return (lambda x, *d: x.permute(*d))(o, 0, 2, 1, 3).reshape(B, S, D) @ wo

    def _B_ref(x, w1, w2, w3, w4):
        B, S, D = x.shape
        h = _gelu(_layernorm(x, axis=-1) @ w1)
        c = D // 4
        g0 = _pad(h[:, :, 0*c:1*c], ((0, 0), (1, 0), (0, 0)))[:, :S, :]
        g1 = _pad(h[:, :, 1*c:2*c], ((0, 0), (0, 1), (0, 0)))[:, 1:, :]
        g2 = _pad(h[:, :, 2*c:3*c], ((0, 0), (2, 0), (0, 0)))[:, :S, :]
        g3 = _pad(h[:, :, 3*c:   ], ((0, 0), (0, 2), (0, 0)))[:, 2:, :]
        hs = torch.concatenate([g0, g1, g2, g3], axis=-1) @ w2
        x1 = x + hs
        h2 = _gelu(_layernorm(x1, axis=-1) @ w3) @ w4
        return x1 + h2

    x = A_x
    xn = _layernorm(x, axis=-1)
    s = _A_ref(xn, A_wqkv, A_wo)
    g = _silu(_B_ref(xn, B_w1, B_w2, B_w3, B_w4))
    return x + s * g
```

```cpp
#include <hip/hip_runtime.h>
#include <hip/hip_bf16.h>
typedef unsigned short u16;
__device__ __forceinline__ float bf2f(u16 b){ return __uint_as_float(((unsigned)b)<<16); }
__device__ __forceinline__ u16 f2bf(float f){ unsigned u=__float_as_uint(f); u += 0x7fffu + ((u>>16)&1u); return (u16)(u>>16); }
__device__ __forceinline__ float rb(float f){ return bf2f(f2bf(f)); }
constexpr int S=2048,D=512,F=1792,H=32,DH=16;

__global__ void ln_k(const u16* in, u16* out){
  int row=blockIdx.x*4+(threadIdx.x>>6), l=threadIdx.x&63;
  float v[8]; float s=0;
  for(int j=0;j<8;j++){ v[j]=bf2f(in[row*D+l*8+j]); s+=v[j]; }
  for(int o=32;o;o>>=1) s+=__shfl_xor(s,o);
  float m=s/D; float q=0;
  for(int j=0;j<8;j++){ float d=v[j]-m; q+=d*d; }
  for(int o=32;o;o>>=1) q+=__shfl_xor(q,o);
  float r=rsqrtf(q/D+1e-5f);
  for(int j=0;j<8;j++) out[row*D+l*8+j]=f2bf((v[j]-m)*r);
}
__global__ void gemm_k(const u16* A,const u16* B,float* C,int M,int N,int K){
  __shared__ float As[16][17], Bs[16][17];
  int tx=threadIdx.x&15, ty=threadIdx.x>>4;
  int r=blockIdx.y*16+ty, c=blockIdx.x*16+tx; float acc=0;
  for(int k0=0;k0<K;k0+=16){
    As[ty][tx]=bf2f(A[r*K+k0+tx]); Bs[ty][tx]=bf2f(B[(k0+ty)*N+c]);
    __syncthreads();
    for(int k=0;k<16;k++) acc+=As[ty][k]*Bs[k][tx];
    __syncthreads();
  }
  C[r*N+c]=acc;
}
__device__ __forceinline__ float gelu(float x){ float u=0.7978845608f*(x+0.044715f*x*x*x); return 0.5f*x*(1.f+tanhf(u)); }
__device__ __forceinline__ float phi(float t){ return (t>0? t+1.f : expf(t))+1e-6f; }
__global__ void kv_k(const float* qkv, float* kv, float* z){
  int h=blockIdx.x, d=threadIdx.x>>4, e=threadIdx.x&15; float a=0, zz=0;
  for(int s=0;s<S;s++){ float pk=rb(phi(rb(qkv[s*1536+512+h*16+d]))); float v=rb(qkv[s*1536+1024+h*16+e]); a+=pk*v; zz+=pk; }
  kv[h*256+d*16+e]=a; if(e==0) z[h*16+d]=zz;
}
__global__ void o_k(const float* qkv,const float* kv,const float* z,u16* o){
  int s=blockIdx.x, c=threadIdx.x, h=c>>4, e=c&15; float num=0, den=0;
  for(int d=0;d<16;d++){ float pq=rb(phi(rb(qkv[s*1536+h*16+d]))); num+=pq*kv[h*256+d*16+e]; den+=pq*z[h*16+d]; }
  o[s*D+c]=f2bf(num/(den+1e-6f));
}
__global__ void shift_k(const float* hpre,u16* hs){
  int s=blockIdx.x,c=threadIdx.x,g=c>>7; int off = g==0?-1: g==1?1: g==2?-2:2; int t=s+off;
  hs[s*D+c] = (t>=0&&t<S)? f2bf(gelu(rb(hpre[t*D+c]))) : (u16)0;
}
__global__ void addx_k(const u16* xn,const float* hs,u16* x1){ int i=blockIdx.x*256+threadIdx.x; x1[i]=f2bf(bf2f(xn[i])+rb(hs[i])); }
__global__ void gelu_k(const float* a,u16* b){ int i=blockIdx.x*256+threadIdx.x; b[i]=f2bf(gelu(rb(a[i]))); }
__global__ void fin_k(const u16* x,const u16* x1,const float* h2,const float* s,u16* out){ int i=blockIdx.x*256+threadIdx.x;
  float y=rb(bf2f(x1[i])+rb(h2[i])); float g=rb(y/(1.f+expf(-y))); out[i]=f2bf(bf2f(x[i])+rb(rb(s[i])*g)); }

extern "C" void kernel_launch(void* const* d_in, const int* in_sizes, int n_in,
                              void* d_out, int out_size, void* d_ws, size_t ws_size,
                              hipStream_t stream) {
  const u16* x=(const u16*)d_in[0]; const u16* wqkv=(const u16*)d_in[1]; const u16* wo=(const u16*)d_in[2];
  const u16* w1=(const u16*)d_in[4]; const u16* w2=(const u16*)d_in[5]; const u16* w3=(const u16*)d_in[6]; const u16* w4=(const u16*)d_in[7];
  char* ws=(char*)d_ws; size_t MB=1<<20;
  u16* xn=(u16*)(ws); u16* xnn=(u16*)(ws+2*MB); float* qkv=(float*)(ws+4*MB); float* t0=(float*)(ws+20*MB); float* t1=(float*)(ws+40*MB);
  u16* o=(u16*)(ws+60*MB); u16* hs=(u16*)(ws+62*MB); u16* x1=(u16*)(ws+64*MB); u16* x1n=(u16*)(ws+66*MB); u16* h2p=(u16*)(ws+68*MB);
  float* kv=(float*)(ws+80*MB); float* z=(float*)(ws+81*MB); float* sb=(float*)(ws+82*MB);
  ln_k<<<S/4,256,0,stream>>>(x,xn); ln_k<<<S/4,256,0,stream>>>(xn,xnn);
  gemm_k<<<dim3(1536/16,S/16),256,0,stream>>>(xn,wqkv,qkv,S,1536,D);
  kv_k<<<H,256,0,stream>>>(qkv,kv,z);
  o_k<<<S,512,0,stream>>>(qkv,kv,z,o);
  gemm_k<<<dim3(D/16,S/16),256,0,stream>>>(o,wo,sb,S,D,D);
  gemm_k<<<dim3(D/16,S/16),256,0,stream>>>(xnn,w1,t0,S,D,D);
  shift_k<<<S,512,0,stream>>>(t0,hs);
  gemm_k<<<dim3(D/16,S/16),256,0,stream>>>(hs,w2,t1,S,D,D);
  addx_k<<<S*D/256,256,0,stream>>>(xn,t1,x1);
  ln_k<<<S/4,256,0,stream>>>(x1,x1n);
  gemm_k<<<dim3(F/16,S/16),256,0,stream>>>(x1n,w3,t0,S,F,D);
  gelu_k<<<S*F/256,256,0,stream>>>(t0,h2p);
  gemm_k<<<dim3(D/16,S/16),256,0,stream>>>(h2p,w4,t1,S,D,F);
  fin_k<<<S*D/256,256,0,stream>>>(x,x1,t1,sb,(u16*)d_out);
}
```

```cpp
#include <hip/hip_runtime.h>
#include <hip/hip_bf16.h>
typedef unsigned short u16;
__device__ __forceinline__ float bf2f(u16 b){ return __uint_as_float(((unsigned)b)<<16); }
__device__ __forceinline__ u16 f2bf(float f){ unsigned u=__float_as_uint(f); u += 0x7fffu + ((u>>16)&1u); return (u16)(u>>16); }
__device__ __forceinline__ float rb(float f){ return bf2f(f2bf(f)); }
constexpr int S=2048,D=512,F=1792,H=32,DH=16;

__global__ void ln_k(const u16* in, u16* out){
  int row=blockIdx.x*4+(threadIdx.x>>6), l=threadIdx.x&63;
  float v[8]; float s=0;
  for(int j=0;j<8;j++){ v[j]=bf2f(in[row*D+l*8+j]); s+=v[j]; }
  for(int o=32;o;o>>=1) s+=__shfl_xor(s,o);
  float m=s/D; float q=0;
  for(int j=0;j<8;j++){ float d=v[j]-m; q+=d*d; }
  for(int o=32;o;o>>=1) q+=__shfl_xor(q,o);
  float r=rsqrtf(q/D+1e-5f);
  for(int j=0;j<8;j++) out[row*D+l*8+j]=f2bf((v[j]-m)*r);
}
__global__ void gemm_k(const u16* A,const u16* B,float* C,int M,int N,int K){
  __shared__ float As[16][17], Bs[16][17];
  int tx=threadIdx.x&15, ty=threadIdx.x>>4;
  int r=blockIdx.y*16+ty, c=blockIdx.x*16+tx; float acc=0;
  for(int k0=0;k0<K;k0+=16){
    As[ty][tx]=bf2f(A[r*K+k0+tx]); Bs[ty][tx]=bf2f(B[(k0+ty)*N+c]);
    __syncthreads();
    for(int k=0;k<16;k++) acc+=As[ty][k]*Bs[k][tx];
    __syncthreads();
  }
  C[r*N+c]=acc;
}
__device__ __forceinline__ float gelu(float x){ float u=0.7978845608f*(x+0.044715f*x*x*x); return 0.5f*x*(1.f+tanhf(u)); }
__device__ __forceinline__ float phi(float t){ return (t>0? t+1.f : expf(t))+1e-6f; }
__global__ void kv_k(const float* qkv, float* kv, float* z){
  int h=blockIdx.x, d=threadIdx.x>>4, e=threadIdx.x&15; float a=0, zz=0;
  for(int s=0;s<S;s++){ float pk=rb(phi(rb(qkv[s*1536+512+h*16+d]))); float v=rb(qkv[s*1536+1024+h*16+e]); a+=pk*v; zz+=pk; }
  kv[h*256+d*16+e]=a; if(e==0) z[h*16+d]=zz;
}
__global__ void o_k(const float* qkv,const float* kv,const float* z,u16* o){
  int s=blockIdx.x, c=threadIdx.x, h=c>>4, e=c&15; float num=0, den=0;
  for(int d=0;d<16;d++){ float pq=rb(phi(rb(qkv[s*1536+h*16+d]))); num+=pq*kv[h*256+d*16+e]; den+=pq*z[h*16+d]; }
  o[s*D+c]=f2bf(num/(den+1e-6f));
}
__global__ void shift_k(const float* hpre,u16* hs){
  int s=blockIdx.x,c=threadIdx.x,g=c>>7; int off = g==0?-1: g==1?1: g==2?-2:2; int t=s+off;
  hs[s*D+c] = (t>=0&&t<S)? f2bf(gelu(rb(hpre[t*D+c]))) : (u16)0;
}
__global__ void addx_k(const u16* xn,const float* hs,u16* x1){ int i=blockIdx.x*256+threadIdx.x; x1[i]=f2bf(bf2f(xn[i])+rb(hs[i])); }
__global__ void gelu_k(const float* a,u16* b){ int i=blockIdx.x*256+threadIdx.x; b[i]=f2bf(gelu(rb(a[i]))); }
__global__ void fin_k(const u16* x,const u16* x1,const float* h2,const float* s,u16* out){ int i=blockIdx.x*256+threadIdx.x;
  float y=rb(bf2f(x1[i])+rb(h2[i])); float g=rb(y/(1.f+expf(-y))); out[i]=f2bf(bf2f(x[i])+rb(rb(s[i])*g)); }

#define PROBE 0
#define WHO -1

#define GAS __attribute__((address_space(1)))
#define LAS __attribute__((address_space(3)))
#define I8 "v_fma_f32 %0, %0, %1, %1\n v_fma_f32 %2, %2, %1, %1\n v_fma_f32 %3, %3, %1, %1\n v_fma_f32 %4, %4, %1, %1\n v_fma_f32 %0, %0, %1, %1\n v_fma_f32 %2, %2, %1, %1\n v_fma_f32 %3, %3, %1, %1\n v_fma_f32 %4, %4, %1, %1\n"
#define I128 I8 I8 I8 I8 I8 I8 I8 I8 I8 I8 I8 I8 I8 I8 I8 I8
__global__ __launch_bounds__(256) void probe_k(const char* src, float* sink){
  __shared__ __attribute__((aligned(16))) char smem[128*1024];
  unsigned long long t0=__builtin_amdgcn_s_memrealtime();
  int bid=blockIdx.x, tid=threadIdx.x; int w=__builtin_amdgcn_readfirstlane(tid>>6);
  int xcd=bid&7, idx=bid>>3;
  __builtin_amdgcn_s_barrier();
  unsigned long long t1=__builtin_amdgcn_s_memrealtime();
  if(WHO<0 || bid==WHO){ unsigned long long dl=(t1-t0)*100; while(__builtin_amdgcn_s_memrealtime()-t1<dl) __builtin_amdgcn_s_sleep(4); }
  if(smem[tid]==123 && t1==5) sink[1]=1;
}
extern "C" void kernel_launch(void* const* d_in, const int* in_sizes, int n_in,
                              void* d_out, int out_size, void* d_ws, size_t ws_size,
                              hipStream_t stream) {
  const u16* x=(const u16*)d_in[0]; const u16* wqkv=(const u16*)d_in[1]; const u16* wo=(const u16*)d_in[2];
  const u16* w1=(const u16*)d_in[4]; const u16* w2=(const u16*)d_in[5]; const u16* w3=(const u16*)d_in[6]; const u16* w4=(const u16*)d_in[7];
  char* ws=(char*)d_ws; size_t MB=1<<20;
  u16* xn=(u16*)(ws); u16* xnn=(u16*)(ws+2*MB); float* qkv=(float*)(ws+4*MB); float* t0=(float*)(ws+20*MB); float* t1=(float*)(ws+40*MB);
  u16* o=(u16*)(ws+60*MB); u16* hs=(u16*)(ws+62*MB); u16* x1=(u16*)(ws+64*MB); u16* x1n=(u16*)(ws+66*MB); u16* h2p=(u16*)(ws+68*MB);
  float* kv=(float*)(ws+80*MB); float* z=(float*)(ws+81*MB); float* sb=(float*)(ws+82*MB);
  probe_k<<<256,256,0,stream>>>((const char*)w3,(float*)(ws+100*MB));
  ln_k<<<S/4,256,0,stream>>>(x,xn); ln_k<<<S/4,256,0,stream>>>(xn,xnn);
  gemm_k<<<dim3(1536/16,S/16),256,0,stream>>>(xn,wqkv,qkv,S,1536,D);
  kv_k<<<H,256,0,stream>>>(qkv,kv,z);
  o_k<<<S,512,0,stream>>>(qkv,kv,z,o);
  gemm_k<<<dim3(D/16,S/16),256,0,stream>>>(o,wo,sb,S,D,D);
  gemm_k<<<dim3(D/16,S/16),256,0,stream>>>(xnn,w1,t0,S,D,D);
  shift_k<<<S,512,0,stream>>>(t0,hs);
  gemm_k<<<dim3(D/16,S/16),256,0,stream>>>(hs,w2,t1,S,D,D);
  addx_k<<<S*D/256,256,0,stream>>>(xn,t1,x1);
  ln_k<<<S/4,256,0,stream>>>(x1,x1n);
  gemm_k<<<dim3(F/16,S/16),256,0,stream>>>(x1n,w3,t0,S,F,D);
  gelu_k<<<S*F/256,256,0,stream>>>(t0,h2p);
  gemm_k<<<dim3(D/16,S/16),256,0,stream>>>(h2p,w4,t1,S,D,F);
  fin_k<<<S*D/256,256,0,stream>>>(x,x1,t1,sb,(u16*)d_out);
}
```

```cpp
#include <hip/hip_runtime.h>
#include <hip/hip_bf16.h>
typedef unsigned short u16;
__device__ __forceinline__ float bf2f(u16 b){ return __uint_as_float(((unsigned)b)<<16); }
__device__ __forceinline__ u16 f2bf(float f){ unsigned u=__float_as_uint(f); u += 0x7fffu + ((u>>16)&1u); return (u16)(u>>16); }
__device__ __forceinline__ float rb(float f){ return bf2f(f2bf(f)); }
constexpr int S=2048,D=512,F=1792,H=32,DH=16;

__global__ void ln_k(const u16* in, u16* out){
  int row=blockIdx.x*4+(threadIdx.x>>6), l=threadIdx.x&63;
  float v[8]; float s=0;
  for(int j=0;j<8;j++){ v[j]=bf2f(in[row*D+l*8+j]); s+=v[j]; }
  for(int o=32;o;o>>=1) s+=__shfl_xor(s,o);
  float m=s/D; float q=0;
  for(int j=0;j<8;j++){ float d=v[j]-m; q+=d*d; }
  for(int o=32;o;o>>=1) q+=__shfl_xor(q,o);
  float r=rsqrtf(q/D+1e-5f);
  for(int j=0;j<8;j++) out[row*D+l*8+j]=f2bf((v[j]-m)*r);
}
__global__ void gemm_k(const u16* A,const u16* B,float* C,int M,int N,int K){
  __shared__ float As[16][17], Bs[16][17];
  int tx=threadIdx.x&15, ty=threadIdx.x>>4;
  int r=blockIdx.y*16+ty, c=blockIdx.x*16+tx; float acc=0;
  for(int k0=0;k0<K;k0+=16){
    As[ty][tx]=bf2f(A[r*K+k0+tx]); Bs[ty][tx]=bf2f(B[(k0+ty)*N+c]);
    __syncthreads();
    for(int k=0;k<16;k++) acc+=As[ty][k]*Bs[k][tx];
    __syncthreads();
  }
  C[r*N+c]=acc;
}
__device__ __forceinline__ float gelu(float x){ float u=0.7978845608f*(x+0.044715f*x*x*x); return 0.5f*x*(1.f+tanhf(u)); }
__device__ __forceinline__ float phi(float t){ return (t>0? t+1.f : expf(t))+1e-6f; }
__global__ void kv_k(const float* qkv, float* kv, float* z){
  int h=blockIdx.x, d=threadIdx.x>>4, e=threadIdx.x&15; float a=0, zz=0;
  for(int s=0;s<S;s++){ float pk=rb(phi(rb(qkv[s*1536+512+h*16+d]))); float v=rb(qkv[s*1536+1024+h*16+e]); a+=pk*v; zz+=pk; }
  kv[h*256+d*16+e]=a; if(e==0) z[h*16+d]=zz;
}
__global__ void o_k(const float* qkv,const float* kv,const float* z,u16* o){
  int s=blockIdx.x, c=threadIdx.x, h=c>>4, e=c&15; float num=0, den=0;
  for(int d=0;d<16;d++){ float pq=rb(phi(rb(qkv[s*1536+h*16+d]))); num+=pq*kv[h*256+d*16+e]; den+=pq*z[h*16+d]; }
  o[s*D+c]=f2bf(num/(den+1e-6f));
}
__global__ void shift_k(const float* hpre,u16* hs){
  int s=blockIdx.x,c=threadIdx.x,g=c>>7; int off = g==0?-1: g==1?1: g==2?-2:2; int t=s+off;
  hs[s*D+c] = (t>=0&&t<S)? f2bf(gelu(rb(hpre[t*D+c]))) : (u16)0;
}
__global__ void addx_k(const u16* xn,const float* hs,u16* x1){ int i=blockIdx.x*256+threadIdx.x; x1[i]=f2bf(bf2f(xn[i])+rb(hs[i])); }
__global__ void gelu_k(const float* a,u16* b){ int i=blockIdx.x*256+threadIdx.x; b[i]=f2bf(gelu(rb(a[i]))); }
__global__ void fin_k(const u16* x,const u16* x1,const float* h2,const float* s,u16* out){ int i=blockIdx.x*256+threadIdx.x;
  float y=rb(bf2f(x1[i])+rb(h2[i])); float g=rb(y/(1.f+expf(-y))); out[i]=f2bf(bf2f(x[i])+rb(rb(s[i])*g)); }

#define PROBE 4
#define WHO -1
#define PFMODE 0

#define GAS __attribute__((address_space(1)))
#define LAS __attribute__((address_space(3)))
typedef unsigned v4u __attribute__((ext_vector_type(4)));
#define I8 "v_fma_f32 %0, %0, %1, %1\n v_fma_f32 %2, %2, %1, %1\n v_fma_f32 %3, %3, %1, %1\n v_fma_f32 %4, %4, %1, %1\n v_fma_f32 %0, %0, %1, %1\n v_fma_f32 %2, %2, %1, %1\n v_fma_f32 %3, %3, %1, %1\n v_fma_f32 %4, %4, %1, %1\n"
#define I128 I8 I8 I8 I8 I8 I8 I8 I8 I8 I8 I8 I8 I8 I8 I8 I8
__global__ __launch_bounds__(256) void probe_k(const char* src, const char* dummy, float* sink, int npass){
  __shared__ __attribute__((aligned(16))) char smem[128*1024];
  unsigned long long t0=__builtin_amdgcn_s_memrealtime();
  int bid=blockIdx.x, tid=threadIdx.x; int w=__builtin_amdgcn_readfirstlane(tid>>6);
  int xcd=bid&7, idx=bid>>3;
  #pragma unroll 1
  for(int p=0;p<npass;p++){
    const char* s = p? src: dummy;
    const char* base; int n;
    if(PROBE==2){ base=s+(size_t)(idx&7)*128*1024; n=32; }
    if(PROBE==3){ base=s+(size_t)(xcd*4+(idx>>3))*64*1024; n=16; }
    if(PROBE==4){ base=s+(size_t)bid*4096; n=1; }
    __builtin_amdgcn_s_barrier();
    t0=__builtin_amdgcn_s_memrealtime();
    asm volatile("s_waitcnt lgkmcnt(0)":::"memory");
    #pragma unroll 1
    for(int i=0;i<n;i++) __builtin_amdgcn_global_load_lds((const GAS void*)(base+(i*4+w)*1024+(tid&63)*16),(LAS void*)(smem+(w*32+i)*1024),16,0,0);
    asm volatile("s_waitcnt vmcnt(0)":::"memory");
  }
  __builtin_amdgcn_s_barrier();
  unsigned long long t1=__builtin_amdgcn_s_memrealtime();
  if(WHO<0 || bid==WHO){ unsigned long long dl=(t1-t0)*100; while(__builtin_amdgcn_s_memrealtime()-t1<dl) __builtin_amdgcn_s_sleep(4); }
  if(smem[tid]==123 && t1==5) sink[1]=1;
}
extern "C" void kernel_launch(void* const* d_in, const int* in_sizes, int n_in,
                              void* d_out, int out_size, void* d_ws, size_t ws_size,
                              hipStream_t stream) {
  const u16* x=(const u16*)d_in[0]; const u16* wqkv=(const u16*)d_in[1]; const u16* wo=(const u16*)d_in[2];
  const u16* w1=(const u16*)d_in[4]; const u16* w2=(const u16*)d_in[5]; const u16* w3=(const u16*)d_in[6]; const u16* w4=(const u16*)d_in[7];
  char* ws=(char*)d_ws; size_t MB=1<<20;
  u16* xn=(u16*)(ws); u16* xnn=(u16*)(ws+2*MB); float* qkv=(float*)(ws+4*MB); float* t0=(float*)(ws+20*MB); float* t1=(float*)(ws+40*MB);
  u16* o=(u16*)(ws+60*MB); u16* hs=(u16*)(ws+62*MB); u16* x1=(u16*)(ws+64*MB); u16* x1n=(u16*)(ws+66*MB); u16* h2p=(u16*)(ws+68*MB);
  float* kv=(float*)(ws+80*MB); float* z=(float*)(ws+81*MB); float* sb=(float*)(ws+82*MB);
  probe_k<<<256,256,0,stream>>>((const char*)w4,(const char*)d_in[3],(float*)(ws+100*MB),2);
  ln_k<<<S/4,256,0,stream>>>(x,xn); ln_k<<<S/4,256,0,stream>>>(xn,xnn);
  gemm_k<<<dim3(1536/16,S/16),256,0,stream>>>(xn,wqkv,qkv,S,1536,D);
  kv_k<<<H,256,0,stream>>>(qkv,kv,z);
  o_k<<<S,512,0,stream>>>(qkv,kv,z,o);
  gemm_k<<<dim3(D/16,S/16),256,0,stream>>>(o,wo,sb,S,D,D);
  gemm_k<<<dim3(D/16,S/16),256,0,stream>>>(xnn,w1,t0,S,D,D);
  shift_k<<<S,512,0,stream>>>(t0,hs);
  gemm_k<<<dim3(D/16,S/16),256,0,stream>>>(hs,w2,t1,S,D,D);
  addx_k<<<S*D/256,256,0,stream>>>(xn,t1,x1);
  ln_k<<<S/4,256,0,stream>>>(x1,x1n);
  gemm_k<<<dim3(F/16,S/16),256,0,stream>>>(x1n,w3,t0,S,F,D);
  gelu_k<<<S*F/256,256,0,stream>>>(t0,h2p);
  gemm_k<<<dim3(D/16,S/16),256,0,stream>>>(h2p,w4,t1,S,D,F);
  fin_k<<<S*D/256,256,0,stream>>>(x,x1,t1,sb,(u16*)d_out);
}
```

```cpp
#include <hip/hip_runtime.h>
#include <hip/hip_bf16.h>

__global__ __launch_bounds__(256, 1) void fused_block_kernel(
    const void* x, unsigned n0, unsigned f0, const void* wqkv, unsigned n1, unsigned f1,
    const void* wo, unsigned n2, unsigned f2, const void* w1, unsigned n3, unsigned f3,
    const void* w2, unsigned n4, unsigned f4, const void* w3, unsigned n5, unsigned f5,
    const void* w4, unsigned n6, unsigned f6, void* out, unsigned n7, unsigned f7,
    void* ws, unsigned n8, unsigned f8) {
#if defined(__HIP_DEVICE_COMPILE__)
  __shared__ __attribute__((aligned(16))) char smem[163840];
  asm volatile("" ::"s"((unsigned)(size_t)smem));
  asm volatile(
      "s_getpc_b64 s[62:63]\n"
      "L_pc:\n"
      "s_load_dwordx16 s[24:39], %0, 0\n"
      "s_load_dwordx16 s[40:55], %0, 64\n"
      "s_load_dwordx4 s[56:59], %0, 128\n"
      "v_lshrrev_b32 v23, 6, %2\n"
      "v_and_b32 v0, 63, %2\n"
      "v_readfirstlane_b32 s5, v23\n"
      "s_and_b32 s14, %1, 7\n"
      "s_lshr_b32 s22, %1, 3\n"
      "v_lshlrev_b32 v201, 7, v0\n"
      "s_lshl_b32 s93, s22, 2\n"
      "s_add_u32 s93, s93, s5\n"
      "s_mul_i32 s21, s93, 384\n"
      "v_add_u32 v23, s21, v201\n"
      "v_min_u32 v23, L_end-L_pc, v23\n"
      "s_mov_b64 exec, 7\n"
      "global_load_dword a255, v23, s[62:63]\n"
      "s_mov_b64 exec, -1\n"
      "s_lshl_b32 s4, s14, 7\n"
      "s_add_u32 s4, s4, s93\n"
      "s_lshr_b32 s9, s4, 5\n"
      "s_mov_b32 s6, s9\n"
      "s_and_b32 s7, s22, 7\n"
      "s_lshl_b32 s72, s6, 2\n"
      "s_add_u32 s72, s72, s5\n"
      "s_lshl_b32 s72, s72, 14\n"
      "s_add_u32 s85, s72, 0x2000\n"
      "s_and_b32 s92, s22, 7\n"
      "v_lshrrev_b32 v224, 3, v0\n"
      "v_and_b32 v225, 7, v0\n"
      "v_lshlrev_b32 v226, 4, v225\n"
      "v_lshl_add_u32 v5, v224, 10, v226\n"
      "v_lshlrev_b32 v21, 17, v0\n"
      "v_lshlrev_b32 v173, 10, v0\n"
      "s_waitcnt lgkmcnt(0)\n"
      "s_sub_u32 s95, 0x180, s48\n"
      "s_and_b32 s95, s95, 0x380\n"
      "s_load_dwordx2 s[60:61], s[56:57], 0\n"
      "s_lshl_b32 s21, s7, 10\n"
      "s_add_u32 s21, s21, s85\n"
      "s_mov_b64 exec, 0xff\n"
      "buffer_load_dword a236, v201, s[24:27], s21 offen\n"
      "s_mov_b64 exec, -1\n"
      "s_mul_i32 s21, s4, 512\n"
      "s_mov_b64 exec, 0xf\n"
      "buffer_load_dword a237, v201, s[36:39], s21 offen\n"
      "s_mov_b64 exec, -1\n"
      "s_mul_i32 s21, s93, 4096\n"
      "s_add_u32 s21, s21, s95\n"
      "s_mov_b64 exec, 0xf\n"
      "buffer_load_dword a238, v173, s[36:39], s21 offen\n"
      "s_mov_b64 exec, -1\n"
      "buffer_load_dwordx4 v[96:99], v5, s[24:27], s72 offen offset:0\n"
      "buffer_load_dwordx4 v[100:103], v5, s[24:27], s72 offen offset:128\n"
      "buffer_load_dwordx4 v[104:107], v5, s[24:27], s72 offen offset:256\n"
      "buffer_load_dwordx4 v[108:111], v5, s[24:27], s72 offen offset:384\n"
      "buffer_load_dwordx4 v[112:115], v5, s[24:27], s72 offen offset:512\n"
      "buffer_load_dwordx4 v[116:119], v5, s[24:27], s72 offen offset:640\n"
      "buffer_load_dwordx4 v[120:123], v5, s[24:27], s72 offen offset:768\n"
      "buffer_load_dwordx4 v[124:127], v5, s[24:27], s72 offen offset:896\n"
      "s_lshr_b32 s8, s95, 7\n"
      "s_add_u32 s8, s8, s7\n"
      "s_add_u32 s8, s8, 1\n"
      "s_and_b32 s8, s8, 7\n"
      "s_and_b32 s10, s5, 1\n"
      "s_lshr_b32 s11, s5, 1\n"
      "s_lshl_b32 s15, s5, 12\n"
      "s_lshl_b32 s22, s8, 7\n"
      "s_lshl_b32 s16, s5, 13\n"
      "s_add_u32 s16, s16, s22\n"
      "s_mul_i32 s17, s5, 24576\n"
      "s_add_u32 s17, s17, s22\n"
      "s_xor_b32 s22, s22, 0\n"
      "s_lshl_b32 s18, s10, 13\n"
      "s_add_u32 s18, s18, s22\n"
      "s_mul_i32 s19, s11, 917504\n"
      "s_add_u32 s19, s19, s18\n"
      "s_lshl_b32 s23, s11, 18\n"
      "s_add_u32 s18, s18, s23\n"
      "buffer_load_dwordx4 v[128:131], v5, s[24:27], s85 offen offset:0\n"
      "buffer_load_dwordx4 v[132:135], v5, s[24:27], s85 offen offset:128\n"
      "buffer_load_dwordx4 v[136:139], v5, s[24:27], s85 offen offset:256\n"
      "buffer_load_dwordx4 v[140:143], v5, s[24:27], s85 offen offset:384\n"
      "s_lshl_b32 s6, s6, 1\n"
      "s_add_u32 s6, s6, s10\n"
      "s_lshl_b32 s7, s8, 1\n"
      "s_add_u32 s7, s7, s11\n"
      "s_lshr_b32 s12, s14, 1\n"
      "s_lshl_b32 s12, s12, 2\n"
      "s_and_b32 s23, s9, 3\n"
      "s_add_u32 s12, s12, s23\n"
      "s_lshl_b32 s12, s12, 2\n"
      "s_add_u32 s12, s12, s5\n"
      "s_and_b32 s23, s14, 1\n"
      "s_mul_i32 s13, s23, 7\n"
      "s_add_u32 s13, s13, s92\n"
      "s_add_u32 s23, s23, 14\n"
      "s_cmp_eq_u32 s92, 7\n"
      "s_cselect_b32 s63, s23, s13\n"
      "s_cselect_b32 s13, 0, s13\n"
      "s_lshl_b32 s23, s12, 4\n"
      "s_add_u32 s63, s63, s23\n"
      "s_lshl_b32 s63, s63, 7\n"
      "s_mul_i32 s20, s5, 28672\n"
      "s_lshl_b32 s23, s13, 8\n"
      "s_add_u32 s20, s20, s23\n"
      "s_lshl_b32 s94, s12, 12\n"
      "s_lshl_b32 s79, s6, 4\n"
      "s_mul_i32 s83, s11, 7\n"
      "s_add_u32 s83, s83, s79\n"
      "s_lshl_b32 s83, s83, 7\n"
      "s_add_u32 s84, s83, 0xc0000\n"
      "s_add_u32 s83, s83, 0xa0000\n"
      "s_add_u32 s79, s79, s7\n"
      "s_lshl_b32 s74, s79, 11\n"
      "s_lshl_b32 s89, s79, 8\n"
      "s_xor_b32 s89, s89, 0x140000\n"
      "s_lshl_b32 s79, s79, 7\n"
      "s_lshl_b32 s73, s6, 1\n"
      "s_add_u32 s73, s73, s11\n"
      "s_mul_i32 s75, s73, 57344\n"
      "s_lshl_b32 s82, s73, 10\n"
      "s_add_u32 s82, s82, 0x60000\n"
      "s_lshl_b32 s73, s73, 14\n"
      "s_mul_i32 s76, s12, 14\n"
      "s_add_u32 s76, s76, s13\n"
      "s_lshl_b32 s76, s76, 13\n"
      "s_lshl_b32 s77, s12, 15\n"
      "s_lshl_b32 s80, s12, 11\n"
      "s_add_u32 s80, s80, 0x40000\n"
      "s_lshl_b32 s81, s7, 8\n"
      "s_add_u32 s81, s81, 0x2000\n"
      "s_lshl_b32 s87, s7, 11\n"
      "s_add_u32 s87, s87, 0x400000\n"
      "s_mov_b32 s64, 0x3f803f80\n"
      "s_mov_b32 s65, 0xffff0000\n"
      "s_mov_b32 s66, 0x3fb8aa3b\n"
      "s_mov_b32 s67, s66\n"
      "s_mov_b32 s68, 0x358637bd\n"
      "s_mov_b32 s69, s68\n"
      "s_mov_b32 s70, 0xffff0000\n"
      "s_mov_b32 s71, 0xffff0000\n"
      "v_lshrrev_b32 v228, 2, v0\n"
      "v_and_b32 v228, 4, v228\n"
      "v_xor_b32 v228, v228, v225\n"
      "v_lshlrev_b32 v228, 4, v228\n"
      "v_lshl_add_u32 v2, v224, 10, v228\n"
      "v_mul_u32_u24 v3, 3072, v224\n"
      "v_add_u32 v3, v3, v228\n"
      "v_mul_u32_u24 v4, 3584, v224\n"
      "v_add_u32 v4, v4, v228\n"
      "v_lshlrev_b32 v1, 4, v0\n"
      "v_lshlrev_b32 v174, 4, v0\n"
      "v_bfe_u32 v230, v0, 2, 2\n"
      "v_and_b32 v231, 3, v0\n"
      "v_lshlrev_b32 v231, 3, v231\n"
      "v_bfe_u32 v232, v0, 4, 1\n"
      "v_lshl_add_u32 v231, v232, 5, v231\n"
      "v_bfe_u32 v232, v0, 3, 1\n"
      "v_lshl_add_u32 v231, v232, 6, v231\n"
      "v_lshl_add_u32 v231, v230, 7, v231\n"
      "v_lshrrev_b32 v172, 5, v0\n"
      "v_and_b32 v171, 31, v0\n"
      "v_lshl_add_u32 v15, v172, 9, v231\n"
      "v_lshl_add_u32 v6, v172, 12, v231\n"
      "s_lshl_b32 s22, s11, 6\n"
      "v_xor_b32 v6, s22, v6\n"
      "v_add_u32 v7, 0x10000, v6\n"
      "v_add_u32 v8, 0x20000, v6\n"
      "v_xor_b32 v169, v224, v225\n"
      "v_lshlrev_b32 v169, 4, v169\n"
      "v_lshl_add_u32 v169, v225, 9, v169\n"
      "s_lshl_b32 s22, s11, 15\n"
      "s_lshl_b32 s23, s10, 8\n"
      "s_add_u32 s22, s22, s23\n"
      "v_add_u32 v169, s22, v169\n"
      "s_lshl_b32 s22, s5, 7\n"
      "s_add_u32 s22, s22, 0x24000\n"
      "v_lshl_add_u32 v170, v224, 3, s22\n"
      "s_lshl_b32 s22, s10, 15\n"
      "v_lshl_add_u32 v232, v172, 9, s22\n"
      "v_xor_b32 v233, v171, v172\n"
      "v_cmp_eq_u32 vcc, 3, v224\n"
      "v_add_u32 v229, 0x1400080, v174\n"
      "s_nop 0\n"
      "v_cndmask_b32 v174, v174, v229, vcc\n"
      "v_xor_b32 v160, 0, v233\n"
      "v_lshl_add_u32 v160, v160, 4, v232\n"
      "v_xor_b32 v161, 2, v233\n"
      "v_lshl_add_u32 v161, v161, 4, v232\n"
      "v_xor_b32 v162, 4, v233\n"
      "v_lshl_add_u32 v162, v162, 4, v232\n"
      "v_xor_b32 v163, 6, v233\n"
      "v_lshl_add_u32 v163, v163, 4, v232\n"
      "s_lshl_b32 s23, s7, 11\n"
      "s_add_u32 s22, s22, s23\n"
      "v_lshl_add_u32 v232, v172, 3, s22\n"
      "s_lshl_b32 s23, s11, 2\n"
      "v_xor_b32 v233, s23, v171\n"
      "v_xor_b32 v164, 0, v233\n"
      "v_lshl_add_u32 v164, v164, 4, v232\n"
      "v_xor_b32 v165, 1, v233\n"
      "v_lshl_add_u32 v165, v165, 4, v232\n"
      "v_xor_b32 v166, 2, v233\n"
      "v_lshl_add_u32 v166, v166, 4, v232\n"
      "v_xor_b32 v167, 3, v233\n"
      "v_lshl_add_u32 v167, v167, 4, v232\n"
      "s_lshl_b32 s22, s10, 8\n"
      "s_add_u32 s22, s22, 0x24000\n"
      "v_lshl_add_u32 v168, v171, 3, s22\n"
      "v_add_u32 v175, s18, v2\n"
      "v_add_u32 v23, s19, v2\n"
      "v_add_u32 v2, s16, v2\n"
      "v_add_u32 v3, s17, v3\n"
      "v_add_u32 v4, s20, v4\n"
      "s_mov_b32 s16, 31744\n"
      "s_mov_b32 s17, 63488\n"
      "s_mov_b32 s18, 95232\n"
      "s_mov_b32 s19, 97280\n"
      "s_mov_b32 s20, 194560\n"
      "s_mov_b32 s12, 291840\n"
      "s_mov_b32 s13, 113664\n"
      "s_mov_b32 s14, 227328\n"
      "s_mov_b32 s2, 340992\n"
      "s_waitcnt vmcnt(11)\n"
      "v_dot2_f32_bf16 v236, v96, s64, 0\n"
      "buffer_load_dwordx4 v[144:147], v5, s[24:27], s85 offen offset:512\n"
      "v_dot2_f32_bf16 v238, v96, v96, 0\n"
      "v_dot2_f32_bf16 v237, v97, s64, 0\n"
      "v_dot2_f32_bf16 v239, v97, v97, 0\n"
      "v_dot2c_f32_bf16 v236, s64, v98\n"
      "v_dot2c_f32_bf16 v238, v98, v98\n"
      "v_dot2c_f32_bf16 v237, s64, v99\n"
      "v_dot2c_f32_bf16 v239, v99, v99\n"
      "s_waitcnt vmcnt(11)\n"
      "v_dot2c_f32_bf16 v236, s64, v100\n"
      "v_dot2c_f32_bf16 v238, v100, v100\n"
      "v_dot2c_f32_bf16 v237, s64, v101\n"
      "v_dot2c_f32_bf16 v239, v101, v101\n"
      "buffer_load_dwordx4 v[148:151], v5, s[24:27], s85 offen offset:640\n"
      "v_dot2c_f32_bf16 v236, s64, v102\n"
      "v_dot2c_f32_bf16 v238, v102, v102\n"
      "v_dot2c_f32_bf16 v237, s64, v103\n"
      "v_dot2c_f32_bf16 v239, v103, v103\n"
      "s_waitcnt vmcnt(11)\n"
      "v_dot2c_f32_bf16 v236, s64, v104\n"
      "v_dot2c_f32_bf16 v238, v104, v104\n"
      "v_dot2c_f32_bf16 v237, s64, v105\n"
      "v_dot2c_f32_bf16 v239, v105, v105\n"
      "v_dot2c_f32_bf16 v236, s64, v106\n"
      "v_dot2c_f32_bf16 v238, v106, v106\n"
      "v_dot2c_f32_bf16 v237, s64, v107\n"
      "buffer_load_dwordx4 v[152:155], v5, s[24:27], s85 offen offset:768\n"
      "v_dot2c_f32_bf16 v239, v107, v107\n"
      "s_waitcnt vmcnt(11)\n"
      "v_dot2c_f32_bf16 v236, s64, v108\n"
      "v_dot2c_f32_bf16 v238, v108, v108\n"
      "v_dot2c_f32_bf16 v237, s64, v109\n"
      "v_dot2c_f32_bf16 v239, v109, v109\n"
      "v_dot2c_f32_bf16 v236, s64, v110\n"
      "v_dot2c_f32_bf16 v238, v110, v110\n"
      "v_dot2c_f32_bf16 v237, s64, v111\n"
      "v_dot2c_f32_bf16 v239, v111, v111\n"
      "s_waitcnt vmcnt(10)\n"
      "v_dot2c_f32_bf16 v236, s64, v112\n"
      "buffer_load_dwordx4 v[156:159], v5, s[24:27], s85 offen offset:896\n"
      "v_dot2c_f32_bf16 v238, v112, v112\n"
      "v_dot2c_f32_bf16 v237, s64, v113\n"
      "v_dot2c_f32_bf16 v239, v113, v113\n"
      "v_dot2c_f32_bf16 v236, s64, v114\n"
      "v_dot2c_f32_bf16 v238, v114, v114\n"
      "v_dot2c_f32_bf16 v237, s64, v115\n"
      "v_dot2c_f32_bf16 v239, v115, v115\n"
      "s_waitcnt vmcnt(10)\n"
      "v_dot2c_f32_bf16 v236, s64, v116\n"
      "v_dot2c_f32_bf16 v238, v116, v116\n"
      "v_dot2c_f32_bf16 v237, s64, v117\n"
      "v_dot2c_f32_bf16 v239, v117, v117\n"
      "s_add_u32 m0, s15, 0x10000\n"
      "s_nop 0\n"
      "buffer_load_dwordx4 v2, s[36:39], 0 offen offset:0 lds\n"
      "v_dot2c_f32_bf16 v236, s64, v118\n"
      "v_dot2c_f32_bf16 v238, v118, v118\n"
      "v_dot2c_f32_bf16 v237, s64, v119\n"
      "v_dot2c_f32_bf16 v239, v119, v119\n"
      "s_waitcnt vmcnt(10)\n"
      "v_dot2c_f32_bf16 v236, s64, v120\n"
      "v_dot2c_f32_bf16 v238, v120, v120\n"
      "v_dot2c_f32_bf16 v237, s64, v121\n"
      "v_dot2c_f32_bf16 v239, v121, v121\n"
      "v_dot2c_f32_bf16 v236, s64, v122\n"
      "v_dot2c_f32_bf16 v238, v122, v122\n"
      "v_dot2c_f32_bf16 v237, s64, v123\n"
      "buffer_load_dwordx4 v2, s[36:39], s16 offen offset:1024 lds\n"
      "v_dot2c_f32_bf16 v239, v123, v123\n"
      "s_waitcnt vmcnt(10)\n"
      "v_dot2c_f32_bf16 v236, s64, v124\n"
      "v_dot2c_f32_bf16 v238, v124, v124\n"
      "v_dot2c_f32_bf16 v237, s64, v125\n"
      "v_dot2c_f32_bf16 v239, v125, v125\n"
      "v_dot2c_f32_bf16 v236, s64, v126\n"
      "v_dot2c_f32_bf16 v238, v126, v126\n"
      "v_dot2c_f32_bf16 v237, s64, v127\n"
      "v_dot2c_f32_bf16 v239, v127, v127\n"
      "s_waitcnt lgkmcnt(0)\n"
      "s_add_u32 s60, s60, 0x7f4a7c15\n"
      "s_addc_u32 s61, s61, 0x9e3779b9\n"
      "v_mov_b32 v26, s60\n"
      "buffer_load_dwordx4 v2, s[36:39], s17 offen offset:2048 lds\n"
      "v_mov_b32 v27, s61\n"
      "v_add_f32 v236, v236, v237\n"
      "v_add_f32 v238, v238, v239\n"
      "s_nop 0\n"
      "v_add_f32_dpp v236, v236, v236 quad_perm:[1,0,3,2] row_mask:0xf bank_mask:0xf\n"
      "v_add_f32_dpp v238, v238, v238 quad_perm:[1,0,3,2] row_mask:0xf bank_mask:0xf\n"
      "s_nop 0\n"
      "v_add_f32_dpp v236, v236, v236 quad_perm:[2,3,0,1] row_mask:0xf bank_mask:0xf\n"
      "v_add_f32_dpp v238, v238, v238 quad_perm:[2,3,0,1] row_mask:0xf bank_mask:0xf\n"
      "s_nop 0\n"
      "v_add_f32_dpp v236, v236, v236 row_half_mirror row_mask:0xf bank_mask:0xf\n"
      "v_add_f32_dpp v238, v238, v238 row_half_mirror row_mask:0xf bank_mask:0xf\n"
      "v_mul_f32 v236, 0x3b000000, v236\n"
      "v_mul_f32 v238, 0x3b000000, v238\n"
      "v_fma_f32 v238, -v236, v236, v238\n"
      "buffer_load_dwordx4 v2, s[36:39], s18 offen offset:3072 lds\n"
      "v_max_f32 v238, 0, v238\n"
      "v_add_f32 v239, 0x3727c5ac, v238\n"
      "v_rsq_f32 v237, v239\n"
      "s_nop 0\n"
      "v_cvt_pk_bf16_f32 v240, v237, 0\n"
      "v_mul_f32 v238, v238, v237\n"
      "v_lshlrev_b32 v241, 16, v240\n"
      "v_mul_f32_e64 v242, -v236, v241\n"
      "v_dot2_f32_bf16 v246, v96, v240, v242\n"
      "v_dot2_f32_bf16 v247, v96, v241, v242\n"
      "v_dot2_f32_bf16 v248, v97, v240, v242\n"
      "v_dot2_f32_bf16 v249, v97, v241, v242\n"
      "s_add_u32 m0, s15, 0x14000\n"
      "v_add_u32 v2, 0x20000, v2\n"
      "buffer_load_dwordx4 v2, s[36:39], 0 offen offset:0 lds\n"
      "v_dot2_f32_bf16 v250, v98, v240, v242\n"
      "v_dot2_f32_bf16 v251, v98, v241, v242\n"
      "v_dot2_f32_bf16 v252, v99, v240, v242\n"
      "v_dot2_f32_bf16 v253, v99, v241, v242\n"
      "v_cvt_pk_bf16_f32 v96, v246, v247\n"
      "v_cvt_pk_bf16_f32 v97, v248, v249\n"
      "v_cvt_pk_bf16_f32 v98, v250, v251\n"
      "v_cvt_pk_bf16_f32 v99, v252, v253\n"
      "v_rcp_f32 v245, v241\n"
      "ds_write_b128 v169, v[96:99] offset:0\n"
      "buffer_load_dwordx4 v2, s[36:39], s16 offen offset:1024 lds\n"
      "v_dot2_f32_bf16 v246, v100, v240, v242\n"
      "v_dot2_f32_bf16 v247, v100, v241, v242\n"
      "v_dot2_f32_bf16 v248, v101, v240, v242\n"
      "v_dot2_f32_bf16 v249, v101, v241, v242\n"
      "v_dot2_f32_bf16 v250, v102, v240, v242\n"
      "v_dot2_f32_bf16 v251, v102, v241, v242\n"
      "v_dot2_f32_bf16 v252, v103, v240, v242\n"
      "v_dot2_f32_bf16 v253, v103, v241, v242\n"
      "v_cvt_pk_bf16_f32 v100, v246, v247\n"
      "v_cvt_pk_bf16_f32 v101, v248, v249\n"
      "v_cvt_pk_bf16_f32 v102, v250, v251\n"
      "v_cvt_pk_bf16_f32 v103, v252, v253\n"
      "buffer_load_dwordx4 v2, s[36:39], s17 offen offset:2048 lds\n"
      "v_mul_f32 v238, v238, v237\n"
      "ds_write_b128 v169, v[100:103] offset:4096\n"
      "v_dot2_f32_bf16 v246, v104, v240, v242\n"
      "v_dot2_f32_bf16 v247, v104, v241, v242\n"
      "v_dot2_f32_bf16 v248, v105, v240, v242\n"
      "buffer_load_dwordx4 v2, s[36:39], s18 offen offset:3072 lds\n"
      "v_dot2_f32_bf16 v249, v105, v241, v242\n"
      "v_dot2_f32_bf16 v250, v106, v240, v242\n"
      "v_dot2_f32_bf16 v251, v106, v241, v242\n"
      "v_dot2_f32_bf16 v252, v107, v240, v242\n"
      "v_dot2_f32_bf16 v253, v107, v241, v242\n"
      "v_cvt_pk_bf16_f32 v104, v246, v247\n"
      "v_cvt_pk_bf16_f32 v105, v248, v249\n"
      "v_cvt_pk_bf16_f32 v106, v250, v251\n"
      "v_cvt_pk_bf16_f32 v107, v252, v253\n"
      "v_add_f32 v238, 0x3727c5ac, v238\n"
      "ds_write_b128 v169, v[104:107] offset:8192\n"
      "s_mul_i32 s21, s4, 1536\n"
      "s_mov_b64 exec, 0xfff\n"
      "buffer_load_dword a239, v201, s[28:31], s21 offen\n"
      "s_mov_b64 exec, -1\n"
      "v_dot2_f32_bf16 v246, v108, v240, v242\n"
      "v_dot2_f32_bf16 v247, v108, v241, v242\n"
      "v_dot2_f32_bf16 v248, v109, v240, v242\n"
      "v_dot2_f32_bf16 v249, v109, v241, v242\n"
      "v_dot2_f32_bf16 v250, v110, v240, v242\n"
      "v_dot2_f32_bf16 v251, v110, v241, v242\n"
      "v_dot2_f32_bf16 v252, v111, v240, v242\n"
      "v_dot2_f32_bf16 v253, v111, v241, v242\n"
      "v_cvt_pk_bf16_f32 v108, v246, v247\n"
      "v_cvt_pk_bf16_f32 v109, v248, v249\n"
      "v_cvt_pk_bf16_f32 v110, v250, v251\n"
      "s_mul_i32 s21, s4, 512\n"
      "s_mov_b64 exec, 0xf\n"
      "buffer_load_dword a240, v201, s[40:43], s21 offen\n"
      "s_mov_b64 exec, -1\n"
      "v_cvt_pk_bf16_f32 v111, v252, v253\n"
      "v_rsq_f32 v244, v238\n"
      "ds_write_b128 v169, v[108:111] offset:12288\n"
      "s_lshl_b32 s21, s4, 7\n"
      "s_mov_b64 exec, 0x7f\n"
      "buffer_load_dword a241, v21, s[56:59], s21 offen sc1\n"
      "s_mov_b64 exec, -1\n"
      "v_dot2_f32_bf16 v246, v112, v240, v242\n"
      "v_dot2_f32_bf16 v247, v112, v241, v242\n"
      "v_dot2_f32_bf16 v248, v113, v240, v242\n"
      "v_dot2_f32_bf16 v249, v113, v241, v242\n"
      "v_dot2_f32_bf16 v250, v114, v240, v242\n"
      "v_dot2_f32_bf16 v251, v114, v241, v242\n"
      "v_dot2_f32_bf16 v252, v115, v240, v242\n"
      "v_dot2_f32_bf16 v253, v115, v241, v242\n"
      "v_cvt_pk_bf16_f32 v112, v246, v247\n"
      "v_cvt_pk_bf16_f32 v113, v248, v249\n"
      "v_cvt_pk_bf16_f32 v114, v250, v251\n"
      "s_mul_i32 s21, s4, 1792\n"
      "s_mov_b64 exec, 0x3fff\n"
      "buffer_load_dword a242, v201, s[44:47], s21 offen\n"
      "s_mov_b64 exec, -1\n"
      "v_cvt_pk_bf16_f32 v115, v252, v253\n"
      "v_mul_f32 v245, v245, v237\n"
      "ds_write_b128 v169, v[112:115] offset:16384\n"
      "v_dot2_f32_bf16 v246, v116, v240, v242\n"
      "s_mul_i32 s21, s4, 512\n"
      "s_mov_b64 exec, 0xf\n"
      "buffer_load_dword a243, v201, s[32:35], s21 offen\n"
      "s_mov_b64 exec, -1\n"
      "v_dot2_f32_bf16 v247, v116, v241, v242\n"
      "v_dot2_f32_bf16 v248, v117, v240, v242\n"
      "v_dot2_f32_bf16 v249, v117, v241, v242\n"
      "v_dot2_f32_bf16 v250, v118, v240, v242\n"
      "v_dot2_f32_bf16 v251, v118, v241, v242\n"
      "v_dot2_f32_bf16 v252, v119, v240, v242\n"
      "v_dot2_f32_bf16 v253, v119, v241, v242\n"
      "v_cvt_pk_bf16_f32 v116, v246, v247\n"
      "v_cvt_pk_bf16_f32 v117, v248, v249\n"
      "v_cvt_pk_bf16_f32 v118, v250, v251\n"
      "v_cvt_pk_bf16_f32 v119, v252, v253\n"
      "s_mul_i32 s21, s4, 1792\n"
      "s_mov_b64 exec, 0x3fff\n"
      "buffer_load_dword a244, v201, s[48:51], s21 offen\n"
      "s_mov_b64 exec, -1\n"
      "v_mul_f32 v244, v244, v245\n"
      "ds_write_b128 v169, v[116:119] offset:20480\n"
      "v_dot2_f32_bf16 v246, v120, v240, v242\n"
      "v_dot2_f32_bf16 v247, v120, v241, v242\n"
      "v_dot2_f32_bf16 v248, v121, v240, v242\n"
      "v_dot2_f32_bf16 v249, v121, v241, v242\n"
      "v_dot2_f32_bf16 v250, v122, v240, v242\n"
      "v_dot2_f32_bf16 v251, v122, v241, v242\n"
      "v_dot2_f32_bf16 v252, v123, v240, v242\n"
      "v_dot2_f32_bf16 v253, v123, v241, v242\n"
      "v_cvt_pk_bf16_f32 v120, v246, v247\n"
      "v_cvt_pk_bf16_f32 v121, v248, v249\n"
      "v_cvt_pk_bf16_f32 v122, v250, v251\n"
      "v_cvt_pk_bf16_f32 v123, v252, v253\n"
      "ds_write_b64 v170, v[244:245] offset:0\n"
      "ds_write_b128 v169, v[120:123] offset:24576\n"
      "v_dot2_f32_bf16 v246, v124, v240, v242\n"
      "v_dot2_f32_bf16 v247, v124, v241, v242\n"
      "v_dot2_f32_bf16 v248, v125, v240, v242\n"
      "v_dot2_f32_bf16 v249, v125, v241, v242\n"
      "v_dot2_f32_bf16 v250, v126, v240, v242\n"
      "v_dot2_f32_bf16 v251, v126, v241, v242\n"
      "v_dot2_f32_bf16 v252, v127, v240, v242\n"
      "v_dot2_f32_bf16 v253, v127, v241, v242\n"
      "v_cvt_pk_bf16_f32 v124, v246, v247\n"
      "v_cvt_pk_bf16_f32 v125, v248, v249\n"
      "v_cvt_pk_bf16_f32 v126, v250, v251\n"
      "v_cvt_pk_bf16_f32 v127, v252, v253\n"
      "ds_write_b128 v169, v[124:127] offset:28672\n"
      "s_waitcnt vmcnt(21)\n"
      "v_dot2_f32_bf16 v236, v128, s64, 0\n"
      "v_dot2_f32_bf16 v238, v128, v128, 0\n"
      "v_dot2_f32_bf16 v237, v129, s64, 0\n"
      "v_dot2_f32_bf16 v239, v129, v129, 0\n"
      "v_dot2c_f32_bf16 v236, s64, v130\n"
      "v_dot2c_f32_bf16 v238, v130, v130\n"
      "v_dot2c_f32_bf16 v237, s64, v131\n"
      "v_dot2c_f32_bf16 v239, v131, v131\n"
      "s_waitcnt vmcnt(20)\n"
      "v_dot2c_f32_bf16 v236, s64, v132\n"
      "v_dot2c_f32_bf16 v238, v132, v132\n"
      "v_dot2c_f32_bf16 v237, s64, v133\n"
      "v_dot2c_f32_bf16 v239, v133, v133\n"
      "v_dot2c_f32_bf16 v236, s64, v134\n"
      "v_dot2c_f32_bf16 v238, v134, v134\n"
      "v_dot2c_f32_bf16 v237, s64, v135\n"
      "v_dot2c_f32_bf16 v239, v135, v135\n"
      "s_waitcnt vmcnt(19)\n"
      "v_dot2c_f32_bf16 v236, s64, v136\n"
      "v_dot2c_f32_bf16 v238, v136, v136\n"
      "v_dot2c_f32_bf16 v237, s64, v137\n"
      "v_dot2c_f32_bf16 v239, v137, v137\n"
      "v_dot2c_f32_bf16 v236, s64, v138\n"
      "v_dot2c_f32_bf16 v238, v138, v138\n"
      "v_dot2c_f32_bf16 v237, s64, v139\n"
      "v_dot2c_f32_bf16 v239, v139, v139\n"
      "s_waitcnt vmcnt(18)\n"
      "v_dot2c_f32_bf16 v236, s64, v140\n"
      "v_dot2c_f32_bf16 v238, v140, v140\n"
      "v_dot2c_f32_bf16 v237, s64, v141\n"
      "v_dot2c_f32_bf16 v239, v141, v141\n"
      "v_dot2c_f32_bf16 v236, s64, v142\n"
      "v_dot2c_f32_bf16 v238, v142, v142\n"
      "v_dot2c_f32_bf16 v237, s64, v143\n"
      "v_dot2c_f32_bf16 v239, v143, v143\n"
      "s_waitcnt vmcnt(17)\n"
      "v_dot2c_f32_bf16 v236, s64, v144\n"
      "v_dot2c_f32_bf16 v238, v144, v144\n"
      "v_dot2c_f32_bf16 v237, s64, v145\n"
      "v_dot2c_f32_bf16 v239, v145, v145\n"
      "v_dot2c_f32_bf16 v236, s64, v146\n"
      "v_dot2c_f32_bf16 v238, v146, v146\n"
      "v_dot2c_f32_bf16 v237, s64, v147\n"
      "v_dot2c_f32_bf16 v239, v147, v147\n"
      "s_waitcnt vmcnt(16)\n"
      "v_dot2c_f32_bf16 v236, s64, v148\n"
      "v_dot2c_f32_bf16 v238, v148, v148\n"
      "v_dot2c_f32_bf16 v237, s64, v149\n"
      "v_dot2c_f32_bf16 v239, v149, v149\n"
      "v_dot2c_f32_bf16 v236, s64, v150\n"
      "v_dot2c_f32_bf16 v238, v150, v150\n"
      "v_dot2c_f32_bf16 v237, s64, v151\n"
      "v_dot2c_f32_bf16 v239, v151, v151\n"
      "s_waitcnt vmcnt(15)\n"
      "v_dot2c_f32_bf16 v236, s64, v152\n"
      "v_dot2c_f32_bf16 v238, v152, v152\n"
      "v_dot2c_f32_bf16 v237, s64, v153\n"
      "v_dot2c_f32_bf16 v239, v153, v153\n"
      "v_dot2c_f32_bf16 v236, s64, v154\n"
      "v_dot2c_f32_bf16 v238, v154, v154\n"
      "v_dot2c_f32_bf16 v237, s64, v155\n"
      "v_dot2c_f32_bf16 v239, v155, v155\n"
      "s_waitcnt vmcnt(14)\n"
      "v_dot2c_f32_bf16 v236, s64, v156\n"
      "v_dot2c_f32_bf16 v238, v156, v156\n"
      "v_dot2c_f32_bf16 v237, s64, v157\n"
      "v_dot2c_f32_bf16 v239, v157, v157\n"
      "v_dot2c_f32_bf16 v236, s64, v158\n"
      "v_dot2c_f32_bf16 v238, v158, v158\n"
      "v_dot2c_f32_bf16 v237, s64, v159\n"
      "v_dot2c_f32_bf16 v239, v159, v159\n"
      "s_nop 1\n"
      "v_add_f32 v236, v236, v237\n"
      "v_add_f32 v238, v238, v239\n"
      "s_nop 0\n"
      "v_add_f32_dpp v236, v236, v236 quad_perm:[1,0,3,2] row_mask:0xf bank_mask:0xf\n"
      "v_add_f32_dpp v238, v238, v238 quad_perm:[1,0,3,2] row_mask:0xf bank_mask:0xf\n"
      "s_nop 0\n"
      "v_add_f32_dpp v236, v236, v236 quad_perm:[2,3,0,1] row_mask:0xf bank_mask:0xf\n"
      "v_add_f32_dpp v238, v238, v238 quad_perm:[2,3,0,1] row_mask:0xf bank_mask:0xf\n"
      "s_nop 0\n"
      "v_add_f32_dpp v236, v236, v236 row_half_mirror row_mask:0xf bank_mask:0xf\n"
      "v_add_f32_dpp v238, v238, v238 row_half_mirror row_mask:0xf bank_mask:0xf\n"
      "v_mul_f32 v236, 0x3b000000, v236\n"
      "v_mul_f32 v238, 0x3b000000, v238\n"
      "v_fma_f32 v238, -v236, v236, v238\n"
      "v_max_f32 v238, 0, v238\n"
      "v_add_f32 v239, 0x3727c5ac, v238\n"
      "v_rsq_f32 v237, v239\n"
      "s_nop 0\n"
      "v_cvt_pk_bf16_f32 v240, v237, 0\n"
      "v_mul_f32 v238, v238, v237\n"
      "v_lshlrev_b32 v241, 16, v240\n"
      "v_mul_f32_e64 v242, -v236, v241\n"
      "v_dot2_f32_bf16 v246, v128, v240, v242\n"
      "v_dot2_f32_bf16 v247, v128, v241, v242\n"
      "v_dot2_f32_bf16 v248, v129, v240, v242\n"
      "v_dot2_f32_bf16 v249, v129, v241, v242\n"
      "v_dot2_f32_bf16 v250, v130, v240, v242\n"
      "v_dot2_f32_bf16 v251, v130, v241, v242\n"
      "v_dot2_f32_bf16 v252, v131, v240, v242\n"
      "v_dot2_f32_bf16 v253, v131, v241, v242\n"
      "v_cvt_pk_bf16_f32 v128, v246, v247\n"
      "v_cvt_pk_bf16_f32 v129, v248, v249\n"
      "v_cvt_pk_bf16_f32 v130, v250, v251\n"
      "v_cvt_pk_bf16_f32 v131, v252, v253\n"
      "v_rcp_f32 v245, v241\n"
      "ds_write_b128 v169, v[128:131] offset:128\n"
      "v_dot2_f32_bf16 v246, v132, v240, v242\n"
      "v_dot2_f32_bf16 v247, v132, v241, v242\n"
      "v_dot2_f32_bf16 v248, v133, v240, v242\n"
      "v_dot2_f32_bf16 v249, v133, v241, v242\n"
      "v_dot2_f32_bf16 v250, v134, v240, v242\n"
      "v_dot2_f32_bf16 v251, v134, v241, v242\n"
      "v_dot2_f32_bf16 v252, v135, v240, v242\n"
      "v_dot2_f32_bf16 v253, v135, v241, v242\n"
      "v_cvt_pk_bf16_f32 v132, v246, v247\n"
      "v_cvt_pk_bf16_f32 v133, v248, v249\n"
      "v_cvt_pk_bf16_f32 v134, v250, v251\n"
      "v_cvt_pk_bf16_f32 v135, v252, v253\n"
      "v_mul_f32 v238, v238, v237\n"
      "ds_write_b128 v169, v[132:135] offset:4224\n"
      "v_dot2_f32_bf16 v246, v136, v240, v242\n"
      "v_dot2_f32_bf16 v247, v136, v241, v242\n"
      "v_dot2_f32_bf16 v248, v137, v240, v242\n"
      "v_dot2_f32_bf16 v249, v137, v241, v242\n"
      "v_dot2_f32_bf16 v250, v138, v240, v242\n"
      "v_dot2_f32_bf16 v251, v138, v241, v242\n"
      "v_dot2_f32_bf16 v252, v139, v240, v242\n"
      "v_dot2_f32_bf16 v253, v139, v241, v242\n"
      "v_cvt_pk_bf16_f32 v136, v246, v247\n"
      "v_cvt_pk_bf16_f32 v137, v248, v249\n"
      "v_cvt_pk_bf16_f32 v138, v250, v251\n"
      "v_cvt_pk_bf16_f32 v139, v252, v253\n"
      "v_add_f32 v238, 0x3727c5ac, v238\n"
      "ds_write_b128 v169, v[136:139] offset:8320\n"
      "v_dot2_f32_bf16 v246, v140, v240, v242\n"
      "v_dot2_f32_bf16 v247, v140, v241, v242\n"
      "v_dot2_f32_bf16 v248, v141, v240, v242\n"
      "v_dot2_f32_bf16 v249, v141, v241, v242\n"
      "v_dot2_f32_bf16 v250, v142, v240, v242\n"
      "v_dot2_f32_bf16 v251, v142, v241, v242\n"
      "v_dot2_f32_bf16 v252, v143, v240, v242\n"
      "v_dot2_f32_bf16 v253, v143, v241, v242\n"
      "v_cvt_pk_bf16_f32 v140, v246, v247\n"
      "v_cvt_pk_bf16_f32 v141, v248, v249\n"
      "v_cvt_pk_bf16_f32 v142, v250, v251\n"
      "v_cvt_pk_bf16_f32 v143, v252, v253\n"
      "v_rsq_f32 v244, v238\n"
      "ds_write_b128 v169, v[140:143] offset:12416\n"
      "v_dot2_f32_bf16 v246, v144, v240, v242\n"
      "v_dot2_f32_bf16 v247, v144, v241, v242\n"
      "v_dot2_f32_bf16 v248, v145, v240, v242\n"
      "v_dot2_f32_bf16 v249, v145, v241, v242\n"
      "v_dot2_f32_bf16 v250, v146, v240, v242\n"
      "v_dot2_f32_bf16 v251, v146, v241, v242\n"
      "v_dot2_f32_bf16 v252, v147, v240, v242\n"
      "v_dot2_f32_bf16 v253, v147, v241, v242\n"
      "v_cvt_pk_bf16_f32 v144, v246, v247\n"
      "v_cvt_pk_bf16_f32 v145, v248, v249\n"
      "v_cvt_pk_bf16_f32 v146, v250, v251\n"
      "v_cvt_pk_bf16_f32 v147, v252, v253\n"
      "v_mul_f32 v245, v245, v237\n"
      "ds_write_b128 v169, v[144:147] offset:16512\n"
      "v_dot2_f32_bf16 v246, v148, v240, v242\n"
      "v_dot2_f32_bf16 v247, v148, v241, v242\n"
      "v_dot2_f32_bf16 v248, v149, v240, v242\n"
      "v_dot2_f32_bf16 v249, v149, v241, v242\n"
      "v_dot2_f32_bf16 v250, v150, v240, v242\n"
      "v_dot2_f32_bf16 v251, v150, v241, v242\n"
      "v_dot2_f32_bf16 v252, v151, v240, v242\n"
      "v_dot2_f32_bf16 v253, v151, v241, v242\n"
      "v_cvt_pk_bf16_f32 v148, v246, v247\n"
      "v_cvt_pk_bf16_f32 v149, v248, v249\n"
      "v_cvt_pk_bf16_f32 v150, v250, v251\n"
      "v_cvt_pk_bf16_f32 v151, v252, v253\n"
      "v_mul_f32 v244, v244, v245\n"
      "ds_write_b128 v169, v[148:151] offset:20608\n"
      "v_dot2_f32_bf16 v246, v152, v240, v242\n"
      "v_dot2_f32_bf16 v247, v152, v241, v242\n"
      "v_dot2_f32_bf16 v248, v153, v240, v242\n"
      "v_dot2_f32_bf16 v249, v153, v241, v242\n"
      "v_dot2_f32_bf16 v250, v154, v240, v242\n"
      "v_dot2_f32_bf16 v251, v154, v241, v242\n"
      "v_dot2_f32_bf16 v252, v155, v240, v242\n"
      "v_dot2_f32_bf16 v253, v155, v241, v242\n"
      "v_cvt_pk_bf16_f32 v152, v246, v247\n"
      "v_cvt_pk_bf16_f32 v153, v248, v249\n"
      "v_cvt_pk_bf16_f32 v154, v250, v251\n"
      "v_cvt_pk_bf16_f32 v155, v252, v253\n"
      "ds_write_b64 v170, v[244:245] offset:64\n"
      "ds_write_b128 v169, v[152:155] offset:24704\n"
      "v_dot2_f32_bf16 v246, v156, v240, v242\n"
      "v_dot2_f32_bf16 v247, v156, v241, v242\n"
      "v_dot2_f32_bf16 v248, v157, v240, v242\n"
      "v_dot2_f32_bf16 v249, v157, v241, v242\n"
      "v_dot2_f32_bf16 v250, v158, v240, v242\n"
      "v_dot2_f32_bf16 v251, v158, v241, v242\n"
      "v_dot2_f32_bf16 v252, v159, v240, v242\n"
      "v_dot2_f32_bf16 v253, v159, v241, v242\n"
      "v_cvt_pk_bf16_f32 v156, v246, v247\n"
      "v_cvt_pk_bf16_f32 v157, v248, v249\n"
      "v_cvt_pk_bf16_f32 v158, v250, v251\n"
      "v_cvt_pk_bf16_f32 v159, v252, v253\n"
      "ds_write_b128 v169, v[156:159] offset:28800\n"
      "s_lshl_b32 s22, s8, 7\n"
      "s_add_u32 s72, s72, s22\n"
      "s_add_u32 s85, s85, s22\n"
      "s_waitcnt vmcnt(6) lgkmcnt(0)\n"
      "s_barrier\n"
      "s_add_u32 m0, s15, 0x18000\n"
      "v_add_u32 v2, 0x20000, v2\n"
      "buffer_load_dwordx4 v2, s[36:39], 0 offen offset:0 lds\n"
      "buffer_load_dwordx4 v2, s[36:39], s16 offen offset:1024 lds\n"
      "buffer_load_dwordx4 v2, s[36:39], s17 offen offset:2048 lds\n"
      "buffer_load_dwordx4 v2, s[36:39], s18 offen offset:3072 lds\n"
      "s_add_u32 m0, s15, 0x1c000\n"
      "v_add_u32 v2, 0x20000, v2\n"
      "buffer_load_dwordx4 v2, s[36:39], 0 offen offset:0 lds\n"
      "buffer_load_dwordx4 v2, s[36:39], s16 offen offset:1024 lds\n"
      "buffer_load_dwordx4 v2, s[36:39], s17 offen offset:2048 lds\n"
      "buffer_load_dwordx4 v2, s[36:39], s18 offen offset:3072 lds\n"
      "ds_read_b128 a[0:3], v160 offset:0\n"
      "ds_read_b64_tr_b16 a[192:193], v7 offset:0\n"
      "ds_read_b64_tr_b16 a[194:195], v7 offset:512\n"
      "ds_read_b128 a[4:7], v161 offset:1024\n"
      "ds_read_b64_tr_b16 a[196:197], v7 offset:8192\n"
      "ds_read_b64_tr_b16 a[198:199], v7 offset:8704\n"
      "ds_read_b128 a[8:11], v162 offset:2048\n"
      "ds_read_b64_tr_b16 a[200:201], v7 offset:1024\n"
      "ds_read_b64_tr_b16 a[202:203], v7 offset:1536\n"
      "ds_read_b128 a[12:15], v163 offset:3072\n"
      "ds_read_b64_tr_b16 a[204:205], v7 offset:9216\n"
      "ds_read_b64_tr_b16 a[206:207], v7 offset:9728\n"
      "ds_read_b128 a[16:19], v160 offset:4096\n"
      "s_mul_i32 s21, s93, 12288\n"
      "s_add_u32 s21, s21, s95\n"
      "s_mov_b64 exec, 0xfff\n"
      "buffer_load_dword a245, v173, s[28:31], s21 offen\n"
      "s_mov_b64 exec, -1\n"
      "ds_read_b64_tr_b16 a[208:209], v7 offset:2048\n"
      "ds_read_b64_tr_b16 a[210:211], v7 offset:2560\n"
      "ds_read_b64 v[184:185], v168\n"
      "ds_read_b64 v[176:177], v164 offset:0\n"
      "ds_read_b64 v[178:179], v165 offset:512\n"
      "ds_read_b64 v[180:181], v166 offset:1024\n"
      "ds_read_b64 v[182:183], v167 offset:1536\n"
      "s_waitcnt lgkmcnt(14)\n"
      "v_mfma_f32_32x32x16_bf16 v[96:111], a[192:195], a[0:3], 0\n"
      "s_mul_i32 s21, s93, 4096\n"
      "s_add_u32 s21, s21, s95\n"
      "s_mov_b64 exec, 0xf\n"
      "buffer_load_dword a246, v173, s[40:43], s21 offen\n"
      "s_mov_b64 exec, -1\n"
      "v_mfma_f32_32x32x16_bf16 v[96:111], a[196:199], a[4:7], v[96:111]\n"
      "ds_read_b128 a[20:23], v161 offset:5120\n"
      "ds_read_b64_tr_b16 a[212:213], v7 offset:10240\n"
      "ds_read_b64_tr_b16 a[214:215], v7 offset:10752\n"
      "ds_read_b128 a[24:27], v162 offset:6144\n"
      "ds_read_b64_tr_b16 a[216:217], v7 offset:3072\n"
      "ds_read_b64_tr_b16 a[218:219], v7 offset:3584\n"
      "s_waitcnt lgkmcnt(14)\n"
      "v_mfma_f32_32x32x16_bf16 v[96:111], a[200:203], a[8:11], v[96:111]\n"
      "s_add_u32 m0, s15, 0x20000\n"
      "v_add_u32 v3, 0x400, v3\n"
      "buffer_load_dwordx4 v3, s[28:31], 0 offen offset:0 lds\n"
      "v_mfma_f32_32x32x16_bf16 v[96:111], a[204:207], a[12:15], v[96:111]\n"
      "ds_read_b128 a[28:31], v163 offset:7168\n"
      "ds_read_b64_tr_b16 a[220:221], v7 offset:11264\n"
      "ds_read_b64_tr_b16 a[222:223], v7 offset:11776\n"
      "ds_read_b128 a[32:35], v160 offset:8192\n"
      "ds_read_b64_tr_b16 a[192:193], v7 offset:16384\n"
      "ds_read_b64_tr_b16 a[194:195], v7 offset:16896\n"
      "s_waitcnt lgkmcnt(14)\n"
      "v_mfma_f32_32x32x16_bf16 v[96:111], a[208:211], a[16:19], v[96:111]\n"
      "buffer_load_dwordx4 v3, s[28:31], s19 offen offset:1024 lds\n"
      "s_waitcnt lgkmcnt(6)\n"
      "v_mfma_f32_32x32x16_bf16 v[96:111], a[212:215], a[20:23], v[96:111]\n"
      "ds_read_b128 a[36:39], v161 offset:9216\n"
      "ds_read_b64_tr_b16 a[196:197], v7 offset:24576\n"
      "ds_read_b64_tr_b16 a[198:199], v7 offset:25088\n"
      "ds_read_b128 a[40:43], v162 offset:10240\n"
      "ds_read_b64_tr_b16 a[200:201], v7 offset:17408\n"
      "ds_read_b64_tr_b16 a[202:203], v7 offset:17920\n"
      "v_mfma_f32_32x32x16_bf16 v[96:111], a[216:219], a[24:27], v[96:111]\n"
      "buffer_load_dwordx4 v3, s[28:31], s20 offen offset:2048 lds\n"
      "s_waitcnt lgkmcnt(6)\n"
      "v_mfma_f32_32x32x16_bf16 v[96:111], a[220:223], a[28:31], v[96:111]\n"
      "ds_read_b128 a[44:47], v163 offset:11264\n"
      "ds_read_b64_tr_b16 a[204:205], v7 offset:25600\n"
      "ds_read_b64_tr_b16 a[206:207], v7 offset:26112\n"
      "ds_read_b128 a[48:51], v160 offset:12288\n"
      "ds_read_b64_tr_b16 a[208:209], v7 offset:18432\n"
      "ds_read_b64_tr_b16 a[210:211], v7 offset:18944\n"
      "v_mfma_f32_32x32x16_bf16 v[96:111], a[192:195], a[32:35], v[96:111]\n"
      "buffer_load_dwordx4 v3, s[28:31], s12 offen offset:3072 lds\n"
      "s_waitcnt lgkmcnt(6)\n"
      "v_mfma_f32_32x32x16_bf16 v[96:111], a[196:199], a[36:39], v[96:111]\n"
      "ds_read_b128 a[52:55], v161 offset:13312\n"
      "ds_read_b64_tr_b16 a[212:213], v7 offset:26624\n"
      "ds_read_b64_tr_b16 a[214:215], v7 offset:27136\n"
      "ds_read_b128 a[56:59], v162 offset:14336\n"
      "ds_read_b64_tr_b16 a[216:217], v7 offset:19456\n"
      "ds_read_b64_tr_b16 a[218:219], v7 offset:19968\n"
      "v_mfma_f32_32x32x16_bf16 v[96:111], a[200:203], a[40:43], v[96:111]\n"
      "ds_read_b128 a[60:63], v163 offset:15360\n"
      "ds_read_b64_tr_b16 a[220:221], v7 offset:27648\n"
      "ds_read_b64_tr_b16 a[222:223], v7 offset:28160\n"
      "s_waitcnt lgkmcnt(12)\n"
      "v_mfma_f32_32x32x16_bf16 v[96:111], a[204:207], a[44:47], v[96:111]\n"
      "s_waitcnt lgkmcnt(9)\n"
      "v_mfma_f32_32x32x16_bf16 v[96:111], a[208:211], a[48:51], v[96:111]\n"
      "s_waitcnt lgkmcnt(6)\n"
      "v_mfma_f32_32x32x16_bf16 v[96:111], a[212:215], a[52:55], v[96:111]\n"
      "s_waitcnt vmcnt(6) lgkmcnt(0)\n"
      "s_barrier\n"
      "ds_read_b128 a[64:67], v160 offset:16384\n"
      "ds_read_b64_tr_b16 a[192:193], v7 offset:32768\n"
      "ds_read_b64_tr_b16 a[194:195], v7 offset:33280\n"
      "s_add_u32 m0, s15, 0x10000\n"
      "v_add_u32 v3, 0x60000, v3\n"
      "buffer_load_dwordx4 v3, s[28:31], 0 offen offset:0 lds\n"
      "v_mfma_f32_32x32x16_bf16 v[96:111], a[216:219], a[56:59], v[96:111]\n"
      "ds_read_b128 a[68:71], v161 offset:17408\n"
      "ds_read_b64_tr_b16 a[196:197], v7 offset:40960\n"
      "ds_read_b64_tr_b16 a[198:199], v7 offset:41472\n"
      "ds_read_b128 a[72:75], v162 offset:18432\n"
      "ds_read_b64_tr_b16 a[200:201], v7 offset:33792\n"
      "ds_read_b64_tr_b16 a[202:203], v7 offset:34304\n"
      "v_mfma_f32_32x32x16_bf16 v[96:111], a[220:223], a[60:63], v[96:111]\n"
      "buffer_load_dwordx4 v3, s[28:31], s19 offen offset:1024 lds\n"
      "s_waitcnt lgkmcnt(6)\n"
      "v_mfma_f32_32x32x16_bf16 v[96:111], a[192:195], a[64:67], v[96:111]\n"
      "ds_read_b128 a[76:79], v163 offset:19456\n"
      "ds_read_b64_tr_b16 a[204:205], v7 offset:41984\n"
      "ds_read_b64_tr_b16 a[206:207], v7 offset:42496\n"
      "ds_read_b128 a[80:83], v160 offset:20480\n"
      "ds_read_b64_tr_b16 a[208:209], v7 offset:34816\n"
      "ds_read_b64_tr_b16 a[210:211], v7 offset:35328\n"
      "s_waitcnt lgkmcnt(9)\n"
      "v_mfma_f32_32x32x16_bf16 v[96:111], a[196:199], a[68:71], v[96:111]\n"
      "buffer_load_dwordx4 v3, s[28:31], s20 offen offset:2048 lds\n"
      "s_waitcnt lgkmcnt(6)\n"
      "v_mfma_f32_32x32x16_bf16 v[96:111], a[200:203], a[72:75], v[96:111]\n"
      "ds_read_b128 a[84:87], v161 offset:21504\n"
      "ds_read_b64_tr_b16 a[212:213], v7 offset:43008\n"
      "ds_read_b64_tr_b16 a[214:215], v7 offset:43520\n"
      "ds_read_b128 a[88:91], v162 offset:22528\n"
      "ds_read_b64_tr_b16 a[216:217], v7 offset:35840\n"
      "ds_read_b64_tr_b16 a[218:219], v7 offset:36352\n"
      "s_waitcnt lgkmcnt(9)\n"
      "v_mfma_f32_32x32x16_bf16 v[96:111], a[204:207], a[76:79], v[96:111]\n"
      "buffer_load_dwordx4 v3, s[28:31], s12 offen offset:3072 lds\n"
      "s_waitcnt lgkmcnt(6)\n"
      "v_mfma_f32_32x32x16_bf16 v[96:111], a[208:211], a[80:83], v[96:111]\n"
      "ds_read_b128 a[92:95], v163 offset:23552\n"
      "ds_read_b64_tr_b16 a[220:221], v7 offset:44032\n"
      "ds_read_b64_tr_b16 a[222:223], v7 offset:44544\n"
      "ds_read_b128 a[96:99], v160 offset:24576\n"
      "ds_read_b64_tr_b16 a[192:193], v7 offset:49152\n"
      "ds_read_b64_tr_b16 a[194:195], v7 offset:49664\n"
      "s_waitcnt lgkmcnt(9)\n"
      "v_mfma_f32_32x32x16_bf16 v[96:111], a[212:215], a[84:87], v[96:111]\n"
      "s_add_u32 m0, s15, 0x14000\n"
      "v_add_u32 v3, 0x60000, v3\n"
      "buffer_load_dwordx4 v3, s[28:31], 0 offen offset:0 lds\n"
      "s_waitcnt lgkmcnt(6)\n"
      "v_mfma_f32_32x32x16_bf16 v[96:111], a[216:219], a[88:91], v[96:111]\n"
      "ds_read_b128 a[100:103], v161 offset:25600\n"
      "ds_read_b64_tr_b16 a[196:197], v7 offset:57344\n"
      "ds_read_b64_tr_b16 a[198:199], v7 offset:57856\n"
      "ds_read_b128 a[104:107], v162 offset:26624\n"
      "ds_read_b64_tr_b16 a[200:201], v7 offset:50176\n"
      "ds_read_b64_tr_b16 a[202:203], v7 offset:50688\n"
      "s_waitcnt lgkmcnt(9)\n"
      "v_mfma_f32_32x32x16_bf16 v[96:111], a[220:223], a[92:95], v[96:111]\n"
      "buffer_load_dwordx4 v3, s[28:31], s19 offen offset:1024 lds\n"
      "s_lshr_b32 s22, s8, 2\n"
      "s_add_u32 s22, s22, 1\n"
      "s_sub_u32 s23, 0, s22\n"
      "s_bitcmp1_b32 s8, 1\n"
      "s_cselect_b32 s22, s23, s22\n"
      "s_waitcnt lgkmcnt(6)\n"
      "v_mfma_f32_32x32x16_bf16 v[96:111], a[192:195], a[96:99], v[96:111]\n"
      "ds_read_b128 a[108:111], v163 offset:27648\n"
      "ds_read_b64_tr_b16 a[204:205], v7 offset:58368\n"
      "ds_read_b64_tr_b16 a[206:207], v7 offset:58880\n"
      "ds_read_b128 a[112:115], v160 offset:28672\n"
      "ds_read_b64_tr_b16 a[208:209], v7 offset:51200\n"
      "ds_read_b64_tr_b16 a[210:211], v7 offset:51712\n"
      "s_lshl_b32 s23, s6, 5\n"
      "s_add_u32 s22, s22, s23\n"
      "buffer_load_dwordx4 v3, s[28:31], s20 offen offset:2048 lds\n"
      "s_waitcnt lgkmcnt(6)\n"
      "v_mfma_f32_32x32x16_bf16 v[96:111], a[196:199], a[100:103], v[96:111]\n"
      "ds_read_b128 a[116:119], v161 offset:29696\n"
      "ds_read_b64_tr_b16 a[212:213], v7 offset:59392\n"
      "ds_read_b64_tr_b16 a[214:215], v7 offset:59904\n"
      "v_add_u32 v252, s22, v171\n"
      "v_cmp_gt_u32 vcc, 0x800, v252\n"
      "v_mfma_f32_32x32x16_bf16 v[96:111], a[200:203], a[104:107], v[96:111]\n"
      "buffer_load_dwordx4 v3, s[28:31], s12 offen offset:3072 lds\n"
      "v_bfe_u32 v253, v252, 5, 6\n"
      "v_and_b32 v252, 31, v252\n"
      "s_waitcnt lgkmcnt(3)\n"
      "v_mfma_f32_32x32x16_bf16 v[96:111], a[204:207], a[108:111], v[96:111]\n"
      "ds_read_b128 a[120:123], v162 offset:30720\n"
      "ds_read_b64_tr_b16 a[216:217], v7 offset:52224\n"
      "ds_read_b64_tr_b16 a[218:219], v7 offset:52736\n"
      "ds_read_b128 a[124:127], v163 offset:31744\n"
      "ds_read_b64_tr_b16 a[220:221], v7 offset:60416\n"
      "ds_read_b64_tr_b16 a[222:223], v7 offset:60928\n"
      "v_lshlrev_b32 v252, 4, v252\n"
      "v_lshl_add_u32 v252, v253, 15, v252\n"
      "v_mfma_f32_32x32x16_bf16 v[96:111], a[208:211], a[112:115], v[96:111]\n"
      "s_add_u32 m0, s15, 0x0\n"
      "v_add_u32 v3, 0x60000, v3\n"
      "buffer_load_dwordx4 v3, s[28:31], 0 offen offset:0 lds\n"
      "v_lshl_add_u32 v21, v172, 9, v252\n"
      "v_rcp_f32 v30, v184\n"
      "s_waitcnt lgkmcnt(6)\n"
      "v_mfma_f32_32x32x16_bf16 v[96:111], a[212:215], a[116:119], v[96:111]\n"
      "s_waitcnt lgkmcnt(0)\n"
      "s_barrier\n"
      "v_mul_f32 v252, v184, v184\n"
      "buffer_load_dwordx4 v3, s[28:31], s19 offen offset:1024 lds\n"
      "v_mul_f32 v28, 0xc0135761, v184\n"
      "v_mfma_f32_32x32x16_bf16 v[96:111], a[216:219], a[120:123], v[96:111]\n"
      "v_mul_f32 v252, v252, v184\n"
      "v_mul_f32 v29, 0xbdd2d3e8, v252\n"
      "v_mov_b32 v252, 0x7f800000\n"
      "v_cndmask_b32 v28, 0, v28, vcc\n"
      "v_cndmask_b32 v29, 0, v29, vcc\n"
      "v_mfma_f32_32x32x16_bf16 v[96:111], a[220:223], a[124:127], v[96:111]\n"
      "v_cndmask_b32 v30, v252, v30, vcc\n"
      "s_nop 10\n"
      "v_pk_mul_f32 v[232:233], v[96:97], v[96:97]\n"
      "buffer_load_dwordx4 v3, s[28:31], s20 offen offset:2048 lds\n"
      "v_pk_mul_f32 v[234:235], v[98:99], v[98:99]\n"
      "v_pk_mul_f32 v[236:237], v[100:101], v[100:101]\n"
      "v_pk_mul_f32 v[238:239], v[102:103], v[102:103]\n"
      "v_pk_fma_f32 v[232:233], v[232:233], v[28:29], v[28:29] op_sel:[0,1,0] op_sel_hi:[1,1,0]\n"
      "v_pk_fma_f32 v[234:235], v[234:235], v[28:29], v[28:29] op_sel:[0,1,0] op_sel_hi:[1,1,0]\n"
      "v_pk_fma_f32 v[236:237], v[236:237], v[28:29], v[28:29] op_sel:[0,1,0] op_sel_hi:[1,1,0]\n"
      "v_pk_fma_f32 v[238:239], v[238:239], v[28:29], v[28:29] op_sel:[0,1,0] op_sel_hi:[1,1,0]\n"
      "v_pk_mul_f32 v[232:233], v[232:233], v[96:97]\n"
      "v_pk_mul_f32 v[234:235], v[234:235], v[98:99]\n"
      "v_pk_mul_f32 v[236:237], v[236:237], v[100:101]\n"
      "v_pk_mul_f32 v[238:239], v[238:239], v[102:103]\n"
      "v_exp_f32 v232, v232\n"
      "buffer_load_dwordx4 v3, s[28:31], s12 offen offset:3072 lds\n"
      "v_exp_f32 v233, v233\n"
      "v_exp_f32 v234, v234\n"
      "v_exp_f32 v235, v235\n"
      "v_exp_f32 v236, v236\n"
      "v_exp_f32 v237, v237\n"
      "v_exp_f32 v238, v238\n"
      "v_exp_f32 v239, v239\n"
      "s_add_u32 m0, s15, 0x8000\n"
      "v_add_u32 v3, 0xffee0400, v3\n"
      "buffer_load_dwordx4 v3, s[28:31], 0 offen offset:0 lds\n"
      "v_pk_fma_f32 v[232:233], v[232:233], v[30:31], v[30:31] op_sel_hi:[1,0,0]\n"
      "v_pk_fma_f32 v[234:235], v[234:235], v[30:31], v[30:31] op_sel_hi:[1,0,0]\n"
      "v_pk_fma_f32 v[236:237], v[236:237], v[30:31], v[30:31] op_sel_hi:[1,0,0]\n"
      "v_pk_fma_f32 v[238:239], v[238:239], v[30:31], v[30:31] op_sel_hi:[1,0,0]\n"
      "v_rcp_f32 v232, v232\n"
      "v_rcp_f32 v233, v233\n"
      "v_rcp_f32 v234, v234\n"
      "v_rcp_f32 v235, v235\n"
      "v_rcp_f32 v236, v236\n"
      "buffer_load_dwordx4 v3, s[28:31], s19 offen offset:1024 lds\n"
      "v_rcp_f32 v237, v237\n"
      "v_rcp_f32 v238, v238\n"
      "v_rcp_f32 v239, v239\n"
      "v_pk_mul_f32 v[232:233], v[232:233], v[96:97]\n"
      "v_pk_mul_f32 v[234:235], v[234:235], v[98:99]\n"
      "v_pk_mul_f32 v[236:237], v[236:237], v[100:101]\n"
      "v_pk_mul_f32 v[238:239], v[238:239], v[102:103]\n"
      "v_cvt_pk_bf16_f32 v224, v232, v233\n"
      "v_cvt_pk_bf16_f32 v225, v234, v235\n"
      "v_cvt_pk_bf16_f32 v226, v236, v237\n"
      "buffer_load_dwordx4 v3, s[28:31], s20 offen offset:2048 lds\n"
      "v_cvt_pk_bf16_f32 v227, v238, v239\n"
      "buffer_store_dwordx4 v[224:227], v21, s[56:59], s87 offen offset:0 sc1\n"
      "v_pk_mul_f32 v[232:233], v[104:105], v[104:105]\n"
      "v_pk_mul_f32 v[234:235], v[106:107], v[106:107]\n"
      "v_pk_mul_f32 v[236:237], v[108:109], v[108:109]\n"
      "v_pk_mul_f32 v[238:239], v[110:111], v[110:111]\n"
      "v_pk_fma_f32 v[232:233], v[232:233], v[28:29], v[28:29] op_sel:[0,1,0] op_sel_hi:[1,1,0]\n"
      "v_pk_fma_f32 v[234:235], v[234:235], v[28:29], v[28:29] op_sel:[0,1,0] op_sel_hi:[1,1,0]\n"
      "v_pk_fma_f32 v[236:237], v[236:237], v[28:29], v[28:29] op_sel:[0,1,0] op_sel_hi:[1,1,0]\n"
      "v_pk_fma_f32 v[238:239], v[238:239], v[28:29], v[28:29] op_sel:[0,1,0] op_sel_hi:[1,1,0]\n"
      "v_pk_mul_f32 v[232:233], v[232:233], v[104:105]\n"
      "v_pk_mul_f32 v[234:235], v[234:235], v[106:107]\n"
      "v_pk_mul_f32 v[236:237], v[236:237], v[108:109]\n"
      "v_pk_mul_f32 v[238:239], v[238:239], v[110:111]\n"
      "v_exp_f32 v232, v232\n"
      "buffer_load_dwordx4 v3, s[28:31], s12 offen offset:3072 lds\n"
      "v_exp_f32 v233, v233\n"
      "v_exp_f32 v234, v234\n"
      "v_exp_f32 v235, v235\n"
      "v_exp_f32 v236, v236\n"
      "v_exp_f32 v237, v237\n"
      "v_exp_f32 v238, v238\n"
      "v_exp_f32 v239, v239\n"
      "s_add_u32 m0, s15, 0x18000\n"
      "v_add_u32 v3, 0x60000, v3\n"
      "buffer_load_dwordx4 v3, s[28:31], 0 offen offset:0 lds\n"
      "v_pk_fma_f32 v[232:233], v[232:233], v[30:31], v[30:31] op_sel_hi:[1,0,0]\n"
      "v_pk_fma_f32 v[234:235], v[234:235], v[30:31], v[30:31] op_sel_hi:[1,0,0]\n"
      "v_pk_fma_f32 v[236:237], v[236:237], v[30:31], v[30:31] op_sel_hi:[1,0,0]\n"
      "v_pk_fma_f32 v[238:239], v[238:239], v[30:31], v[30:31] op_sel_hi:[1,0,0]\n"
      "v_rcp_f32 v232, v232\n"
      "v_rcp_f32 v233, v233\n"
      "v_rcp_f32 v234, v234\n"
      "v_rcp_f32 v235, v235\n"
      "v_rcp_f32 v236, v236\n"
      "buffer_load_dwordx4 v3, s[28:31], s19 offen offset:1024 lds\n"
      "v_rcp_f32 v237, v237\n"
      "v_rcp_f32 v238, v238\n"
      "v_rcp_f32 v239, v239\n"
      "v_pk_mul_f32 v[232:233], v[232:233], v[104:105]\n"
      "v_pk_mul_f32 v[234:235], v[234:235], v[106:107]\n"
      "v_pk_mul_f32 v[236:237], v[236:237], v[108:109]\n"
      "v_pk_mul_f32 v[238:239], v[238:239], v[110:111]\n"
      "v_cvt_pk_bf16_f32 v228, v232, v233\n"
      "v_cvt_pk_bf16_f32 v229, v234, v235\n"
      "v_cvt_pk_bf16_f32 v230, v236, v237\n"
      "buffer_load_dwordx4 v3, s[28:31], s20 offen offset:2048 lds\n"
      "v_cvt_pk_bf16_f32 v231, v238, v239\n"
      "buffer_store_dwordx4 v[228:231], v21, s[56:59], s87 offen offset:1024 sc1\n"
      "s_cmp_eq_u32 s92, 7\n"
      "s_cselect_b32 s46, 0, s46\n"
      "s_waitcnt vmcnt(17)\n"
      "s_barrier\n"
      "ds_read_b64_tr_b16 a[192:193], v8 offset:0\n"
      "ds_read_b64_tr_b16 a[194:195], v8 offset:512\n"
      "buffer_load_dwordx4 v3, s[28:31], s12 offen offset:3072 lds\n"
      "ds_read_b64_tr_b16 a[196:197], v8 offset:8192\n"
      "ds_read_b64_tr_b16 a[198:199], v8 offset:8704\n"
      "ds_read_b64_tr_b16 a[200:201], v8 offset:1024\n"
      "ds_read_b64_tr_b16 a[202:203], v8 offset:1536\n"
      "ds_read_b64_tr_b16 a[204:205], v8 offset:9216\n"
      "ds_read_b64_tr_b16 a[206:207], v8 offset:9728\n"
      "ds_read_b64_tr_b16 a[208:209], v8 offset:2048\n"
      "ds_read_b64_tr_b16 a[210:211], v8 offset:2560\n"
      "s_waitcnt lgkmcnt(8)\n"
      "v_mfma_f32_32x32x16_bf16 v[112:127], a[0:3], a[192:195], 0\n"
      "s_add_u32 m0, s15, 0x1c000\n"
      "v_add_u32 v3, 0x60000, v3\n"
      "buffer_load_dwordx4 v3, s[28:31], 0 offen offset:0 lds\n"
      "s_waitcnt lgkmcnt(6)\n"
      "v_mfma_f32_32x32x16_bf16 v[112:127], a[4:7], a[196:199], v[112:127]\n"
      "ds_read_b64_tr_b16 a[212:213], v8 offset:10240\n"
      "ds_read_b64_tr_b16 a[214:215], v8 offset:10752\n"
      "v_lshlrev_b32 v32, 16, v176\n"
      "ds_read_b64_tr_b16 a[216:217], v8 offset:3072\n"
      "ds_read_b64_tr_b16 a[218:219], v8 offset:3584\n"
      "s_waitcnt lgkmcnt(8)\n"
      "v_mfma_f32_32x32x16_bf16 v[112:127], a[8:11], a[200:203], v[112:127]\n"
      "buffer_load_dwordx4 v3, s[28:31], s19 offen offset:1024 lds\n"
      "v_and_b32 v33, s65, v176\n"
      "s_waitcnt lgkmcnt(4)\n"
      "v_mfma_f32_32x32x16_bf16 v[112:127], a[12:15], a[204:207], v[112:127]\n"
      "ds_read_b64_tr_b16 a[220:221], v8 offset:11264\n"
      "ds_read_b64_tr_b16 a[222:223], v8 offset:11776\n"
      "v_mul_f32 v32, v32, v185\n"
      "v_mul_f32 v33, v33, v185\n"
      "v_lshlrev_b32 v34, 16, v177\n"
      "v_mfma_f32_32x32x16_bf16 v[112:127], a[16:19], a[208:211], v[112:127]\n"
      "buffer_load_dwordx4 v3, s[28:31], s20 offen offset:2048 lds\n"
      "s_waitcnt lgkmcnt(2)\n"
      "v_mfma_f32_32x32x16_bf16 v[112:127], a[20:23], a[212:215], v[112:127]\n"
      "ds_read_b64_tr_b16 a[192:193], v7 offset:0\n"
      "ds_read_b64_tr_b16 a[194:195], v7 offset:512\n"
      "ds_read_b64_tr_b16 a[196:197], v7 offset:8192\n"
      "ds_read_b64_tr_b16 a[198:199], v7 offset:8704\n"
      "v_and_b32 v35, s65, v177\n"
      "v_mul_f32 v34, v34, v185\n"
      "v_mfma_f32_32x32x16_bf16 v[112:127], a[24:27], a[216:219], v[112:127]\n"
      "buffer_load_dwordx4 v3, s[28:31], s12 offen offset:3072 lds\n"
      "v_mul_f32 v35, v35, v185\n"
      "v_lshlrev_b32 v36, 16, v178\n"
      "s_waitcnt lgkmcnt(4)\n"
      "v_mfma_f32_32x32x16_bf16 v[112:127], a[28:31], a[220:223], v[112:127]\n"
      "ds_read_b64_tr_b16 a[200:201], v7 offset:1024\n"
      "ds_read_b64_tr_b16 a[202:203], v7 offset:1536\n"
      "ds_read_b64_tr_b16 a[204:205], v7 offset:9216\n"
      "ds_read_b64_tr_b16 a[206:207], v7 offset:9728\n"
      "v_and_b32 v37, s65, v178\n"
      "s_waitcnt lgkmcnt(6)\n"
      "v_mfma_f32_32x32x16_bf16 v[112:127], a[32:35], a[192:195], v[112:127]\n"
      "s_add_u32 m0, s15, 0x4000\n"
      "v_add_u32 v3, 0x60000, v3\n"
      "buffer_load_dwordx4 v3, s[28:31], 0 offen offset:0 lds\n"
      "v_mul_f32 v36, v36, v185\n"
      "v_mul_f32 v37, v37, v185\n"
      "s_waitcnt lgkmcnt(4)\n"
      "v_mfma_f32_32x32x16_bf16 v[112:127], a[36:39], a[196:199], v[112:127]\n"
      "ds_read_b64_tr_b16 a[208:209], v7 offset:2048\n"
      "ds_read_b64_tr_b16 a[210:211], v7 offset:2560\n"
      "ds_read_b64_tr_b16 a[212:213], v7 offset:10240\n"
      "ds_read_b64_tr_b16 a[214:215], v7 offset:10752\n"
      "v_lshlrev_b32 v38, 16, v179\n"
      "s_waitcnt lgkmcnt(6)\n"
      "v_mfma_f32_32x32x16_bf16 v[112:127], a[40:43], a[200:203], v[112:127]\n"
      "buffer_load_dwordx4 v3, s[28:31], s19 offen offset:1024 lds\n"
      "v_and_b32 v39, s65, v179\n"
      "v_mul_f32 v38, v38, v185\n"
      "s_waitcnt lgkmcnt(4)\n"
      "v_mfma_f32_32x32x16_bf16 v[112:127], a[44:47], a[204:207], v[112:127]\n"
      "ds_read_b64_tr_b16 a[216:217], v7 offset:3072\n"
      "ds_read_b64_tr_b16 a[218:219], v7 offset:3584\n"
      "ds_read_b64_tr_b16 a[220:221], v7 offset:11264\n"
      "ds_read_b64_tr_b16 a[222:223], v7 offset:11776\n"
      "v_mul_f32 v39, v39, v185\n"
      "v_lshlrev_b32 v40, 16, v180\n"
      "s_waitcnt lgkmcnt(6)\n"
      "v_mfma_f32_32x32x16_bf16 v[112:127], a[48:51], a[208:211], v[112:127]\n"
      "buffer_load_dwordx4 v3, s[28:31], s20 offen offset:2048 lds\n"
      "v_and_b32 v41, s65, v180\n"
      "s_waitcnt lgkmcnt(4)\n"
      "v_mfma_f32_32x32x16_bf16 v[112:127], a[52:55], a[212:215], v[112:127]\n"
      "s_waitcnt vmcnt(8)\n"
      "s_mov_b64 exec, 1\n"
      "s_xor_b32 s86, s79, 0x20000\n"
      "buffer_store_dwordx2 v[26:27], v1, s[56:59], s86 offen sc1\n"
      "s_mov_b64 exec, -1\n"
      "s_waitcnt lgkmcnt(0)\n"
      "s_barrier\n"
      "ds_read_b64_tr_b16 a[192:193], v7 offset:16384\n"
      "ds_read_b64_tr_b16 a[194:195], v7 offset:16896\n"
      "ds_read_b64_tr_b16 a[196:197], v7 offset:24576\n"
      "ds_read_b64_tr_b16 a[198:199], v7 offset:25088\n"
      "v_mul_f32 v40, v40, v185\n"
      "v_mul_f32 v41, v41, v185\n"
      "buffer_load_dwordx4 v3, s[28:31], s12 offen offset:3072 lds\n"
      "v_mfma_f32_32x32x16_bf16 v[112:127], a[56:59], a[216:219], v[112:127]\n"
      "ds_read_b64_tr_b16 a[200:201], v7 offset:17408\n"
      "ds_read_b64_tr_b16 a[202:203], v7 offset:17920\n"
      "v_lshlrev_b32 v42, 16, v181\n"
      "ds_read_b64_tr_b16 a[204:205], v7 offset:25600\n"
      "ds_read_b64_tr_b16 a[206:207], v7 offset:26112\n"
      "v_mfma_f32_32x32x16_bf16 v[112:127], a[60:63], a[220:223], v[112:127]\n"
      "s_add_u32 m0, s15, 0xc000\n"
      "s_mov_b32 s16, 15360\n"
      "s_mov_b32 s17, 30720\n"
      "s_mov_b32 s18, 46080\n"
      "buffer_load_dwordx4 v175, s[40:43], 0 offen offset:0 lds\n"
      "v_and_b32 v43, s65, v181\n"
      "v_mul_f32 v42, v42, v185\n"
      "s_waitcnt lgkmcnt(4)\n"
      "v_mfma_f32_32x32x16_bf16 v[112:127], a[64:67], a[192:195], v[112:127]\n"
      "ds_read_b64_tr_b16 a[208:209], v7 offset:18432\n"
      "ds_read_b64_tr_b16 a[210:211], v7 offset:18944\n"
      "v_mul_f32 v43, v43, v185\n"
      "v_lshlrev_b32 v44, 16, v182\n"
      "v_and_b32 v45, s65, v182\n"
      "v_mfma_f32_32x32x16_bf16 v[112:127], a[68:71], a[196:199], v[112:127]\n"
      "buffer_load_dwordx4 v175, s[40:43], s16 offen offset:1024 lds\n"
      "s_waitcnt lgkmcnt(2)\n"
      "v_mfma_f32_32x32x16_bf16 v[112:127], a[72:75], a[200:203], v[112:127]\n"
      "ds_read_b64_tr_b16 a[212:213], v7 offset:26624\n"
      "ds_read_b64_tr_b16 a[214:215], v7 offset:27136\n"
      "ds_read_b64_tr_b16 a[216:217], v7 offset:19456\n"
      "ds_read_b64_tr_b16 a[218:219], v7 offset:19968\n"
      "v_mul_f32 v44, v44, v185\n"
      "v_mul_f32 v45, v45, v185\n"
      "v_mfma_f32_32x32x16_bf16 v[112:127], a[76:79], a[204:207], v[112:127]\n"
      "buffer_load_dwordx4 v175, s[40:43], s17 offen offset:2048 lds\n"
      "v_lshlrev_b32 v46, 16, v183\n"
      "s_waitcnt lgkmcnt(4)\n"
      "v_mfma_f32_32x32x16_bf16 v[112:127], a[80:83], a[208:211], v[112:127]\n"
      "ds_read_b64_tr_b16 a[220:221], v7 offset:27648\n"
      "ds_read_b64_tr_b16 a[222:223], v7 offset:28160\n"
      "ds_read_b64_tr_b16 a[192:193], v6 offset:0\n"
      "ds_read_b64_tr_b16 a[194:195], v6 offset:512\n"
      "v_and_b32 v47, s65, v183\n"
      "v_mul_f32 v46, v46, v185\n"
      "s_waitcnt lgkmcnt(6)\n"
      "v_mfma_f32_32x32x16_bf16 v[112:127], a[84:87], a[212:215], v[112:127]\n"
      "buffer_load_dwordx4 v175, s[40:43], s18 offen offset:3072 lds\n"
      "v_mul_f32 v47, v47, v185\n"
      "v_xor_b32 v18, 64, v15\n"
      "s_waitcnt lgkmcnt(4)\n"
      "v_mfma_f32_32x32x16_bf16 v[112:127], a[88:91], a[216:219], v[112:127]\n"
      "ds_read_b64_tr_b16 a[196:197], v6 offset:8192\n"
      "ds_read_b64_tr_b16 a[198:199], v6 offset:8704\n"
      "ds_read_b64_tr_b16 a[200:201], v6 offset:1024\n"
      "ds_read_b64_tr_b16 a[202:203], v6 offset:1536\n"
      "s_mul_i32 s22, s11, 8256\n"
      "s_waitcnt lgkmcnt(6)\n"
      "v_mfma_f32_32x32x16_bf16 v[112:127], a[92:95], a[220:223], v[112:127]\n"
      "s_add_u32 m0, s15, 0x20000\n"
      "v_add_u32 v175, 0x10000, v175\n"
      "buffer_load_dwordx4 v175, s[40:43], 0 offen offset:0 lds\n"
      "v_xor_b32 v9, s22, v15\n"
      "v_xor_b32 v12, 64, v9\n"
      "s_waitcnt lgkmcnt(4)\n"
      "v_mfma_f32_32x32x16_bf16 v[112:127], a[96:99], a[192:195], v[112:127]\n"
      "ds_read_b64_tr_b16 a[204:205], v6 offset:9216\n"
      "ds_read_b64_tr_b16 a[206:207], v6 offset:9728\n"
      "ds_read_b64_tr_b16 a[208:209], v6 offset:2048\n"
      "ds_read_b64_tr_b16 a[210:211], v6 offset:2560\n"
      "v_add_u32 v10, 0x10000, v9\n"
      "s_waitcnt lgkmcnt(6)\n"
      "v_mfma_f32_32x32x16_bf16 v[112:127], a[100:103], a[196:199], v[112:127]\n"
      "buffer_load_dwordx4 v175, s[40:43], s16 offen offset:1024 lds\n"
      "v_add_u32 v11, 0x20000, v9\n"
      "v_add_u32 v13, 0x10000, v12\n"
      "s_waitcnt lgkmcnt(4)\n"
      "v_mfma_f32_32x32x16_bf16 v[112:127], a[104:107], a[200:203], v[112:127]\n"
      "ds_read_b64_tr_b16 a[212:213], v6 offset:10240\n"
      "ds_read_b64_tr_b16 a[214:215], v6 offset:10752\n"
      "ds_read_b64_tr_b16 a[216:217], v6 offset:3072\n"
      "ds_read_b64_tr_b16 a[218:219], v6 offset:3584\n"
      "v_add_u32 v14, 0x20000, v12\n"
      "v_bfe_u32 v240, v0, 2, 1\n"
      "s_waitcnt lgkmcnt(6)\n"
      "v_mfma_f32_32x32x16_bf16 v[112:127], a[108:111], a[204:207], v[112:127]\n"
      "buffer_load_dwordx4 v175, s[40:43], s17 offen offset:2048 lds\n"
      "v_lshl_add_u32 v240, v240, 1, -1\n"
      "s_waitcnt lgkmcnt(4)\n"
      "v_mfma_f32_32x32x16_bf16 v[112:127], a[112:115], a[208:211], v[112:127]\n"
      "ds_read_b64_tr_b16 a[220:221], v6 offset:11264\n"
      "ds_read_b64_tr_b16 a[222:223], v6 offset:11776\n"
      "v_bfe_u32 v241, v0, 3, 1\n"
      "v_mul_i32_i24 v240, v240, v241\n"
      "v_add_u32 v240, s6, v240\n"
      "s_waitcnt lgkmcnt(4)\n"
      "v_mfma_f32_32x32x16_bf16 v[112:127], a[116:119], a[212:215], v[112:127]\n"
      "s_waitcnt vmcnt(16) lgkmcnt(0)\n"
      "s_barrier\n"
      "ds_read_b64_tr_b16 a[192:193], v6 offset:32768\n"
      "ds_read_b64_tr_b16 a[194:195], v6 offset:33280\n"
      "buffer_load_dwordx4 v175, s[40:43], s18 offen offset:3072 lds\n"
      "v_mfma_f32_32x32x16_bf16 v[112:127], a[120:123], a[216:219], v[112:127]\n"
      "ds_read_b64_tr_b16 a[196:197], v6 offset:40960\n"
      "ds_read_b64_tr_b16 a[198:199], v6 offset:41472\n"
      "ds_read_b64_tr_b16 a[200:201], v6 offset:33792\n"
      "ds_read_b64_tr_b16 a[202:203], v6 offset:34304\n"
      "v_and_b32 v240, 63, v240\n"
      "v_and_b32 v241, 7, v0\n"
      "v_mfma_f32_32x32x16_bf16 v[112:127], a[124:127], a[220:223], v[112:127]\n"
      "s_add_u32 m0, s15, 0x10000\n"
      "v_add_u32 v175, 0x10000, v175\n"
      "buffer_load_dwordx4 v175, s[40:43], 0 offen offset:0 lds\n"
      "v_lshl_add_u32 v240, v240, 4, v241\n"
      "v_lshl_add_u32 v240, s11, 3, v240\n"
      "v_lshlrev_b32 v240, 7, v240\n"
      "v_add_u32 v22, 0x20000, v240\n"
      "s_waitcnt lgkmcnt(4)\n"
      "v_mfma_f32_32x32x16_bf16 v[96:111], a[0:3], a[192:195], 0\n"
      "ds_read_b64_tr_b16 a[204:205], v6 offset:41984\n"
      "ds_read_b64_tr_b16 a[206:207], v6 offset:42496\n"
      "ds_read_b64_tr_b16 a[208:209], v6 offset:34816\n"
      "ds_read_b64_tr_b16 a[210:211], v6 offset:35328\n"
      "s_waitcnt lgkmcnt(6)\n"
      "v_mfma_f32_32x32x16_bf16 v[96:111], a[4:7], a[196:199], v[96:111]\n"
      "buffer_load_dwordx4 v175, s[40:43], s16 offen offset:1024 lds\n"
      "s_waitcnt lgkmcnt(4)\n"
      "v_mfma_f32_32x32x16_bf16 v[96:111], a[8:11], a[200:203], v[96:111]\n"
      "ds_read_b64_tr_b16 a[212:213], v6 offset:43008\n"
      "ds_read_b64_tr_b16 a[214:215], v6 offset:43520\n"
      "ds_read_b64_tr_b16 a[216:217], v6 offset:35840\n"
      "ds_read_b64_tr_b16 a[218:219], v6 offset:36352\n"
      "v_mul_f32 v244, s66, v112\n"
      "s_waitcnt lgkmcnt(6)\n"
      "v_mfma_f32_32x32x16_bf16 v[96:111], a[12:15], a[204:207], v[96:111]\n"
      "buffer_load_dwordx4 v175, s[40:43], s17 offen offset:2048 lds\n"
      "v_mul_f32 v245, s66, v113\n"
      "v_mul_f32 v246, s66, v114\n"
      "s_waitcnt lgkmcnt(4)\n"
      "v_mfma_f32_32x32x16_bf16 v[96:111], a[16:19], a[208:211], v[96:111]\n"
      "ds_read_b64_tr_b16 a[220:221], v6 offset:44032\n"
      "ds_read_b64_tr_b16 a[222:223], v6 offset:44544\n"
      "ds_read_b64_tr_b16 a[192:193], v7 offset:32768\n"
      "ds_read_b64_tr_b16 a[194:195], v7 offset:33280\n"
      "v_mul_f32 v247, s66, v115\n"
      "v_add_f32 v112, s68, v112\n"
      "v_add_f32 v113, s68, v113\n"
      "s_waitcnt lgkmcnt(6)\n"
      "v_mfma_f32_32x32x16_bf16 v[96:111], a[20:23], a[212:215], v[96:111]\n"
      "buffer_load_dwordx4 v175, s[40:43], s18 offen offset:3072 lds\n"
      "v_add_f32 v114, s68, v114\n"
      "v_add_f32 v115, s68, v115\n"
      "v_exp_f32_e64 v244, v244 clamp\n"
      "s_waitcnt lgkmcnt(4)\n"
      "v_mfma_f32_32x32x16_bf16 v[96:111], a[24:27], a[216:219], v[96:111]\n"
      "ds_read_b64_tr_b16 a[196:197], v7 offset:40960\n"
      "ds_read_b64_tr_b16 a[198:199], v7 offset:41472\n"
      "ds_read_b64_tr_b16 a[200:201], v7 offset:33792\n"
      "ds_read_b64_tr_b16 a[202:203], v7 offset:34304\n"
      "v_exp_f32_e64 v245, v245 clamp\n"
      "v_exp_f32_e64 v246, v246 clamp\n"
      "v_exp_f32_e64 v247, v247 clamp\n"
      "s_add_u32 m0, s15, 0x14000\n"
      "v_add_u32 v175, 0x10000, v175\n"
      "buffer_load_dwordx4 v175, s[40:43], 0 offen offset:0 lds\n"
      "s_waitcnt lgkmcnt(4)\n"
      "v_mfma_f32_32x32x16_bf16 v[96:111], a[28:31], a[220:223], v[96:111]\n"
      "ds_read_b64_tr_b16 a[204:205], v7 offset:41984\n"
      "ds_read_b64_tr_b16 a[206:207], v7 offset:42496\n"
      "v_max_f32 v112, s68, v112\n"
      "v_max_f32 v113, s68, v113\n"
      "v_max_f32 v114, s68, v114\n"
      "v_mfma_f32_32x32x16_bf16 v[96:111], a[32:35], a[192:195], v[96:111]\n"
      "buffer_load_dwordx4 v175, s[40:43], s16 offen offset:1024 lds\n"
      "v_max_f32 v115, s68, v115\n"
      "v_add_f32 v112, v112, v244\n"
      "v_add_f32 v113, v113, v245\n"
      "s_waitcnt lgkmcnt(2)\n"
      "v_mfma_f32_32x32x16_bf16 v[96:111], a[36:39], a[196:199], v[96:111]\n"
      "ds_read_b64_tr_b16 a[208:209], v7 offset:34816\n"
      "ds_read_b64_tr_b16 a[210:211], v7 offset:35328\n"
      "ds_read_b64_tr_b16 a[212:213], v7 offset:43008\n"
      "ds_read_b64_tr_b16 a[214:215], v7 offset:43520\n"
      "v_add_f32 v114, v114, v246\n"
      "v_add_f32 v115, v115, v247\n"
      "v_cvt_pk_bf16_f32 v88, v112, v113\n"
      "v_mfma_f32_32x32x16_bf16 v[96:111], a[40:43], a[200:203], v[96:111]\n"
      "buffer_load_dwordx4 v175, s[40:43], s17 offen offset:2048 lds\n"
      "v_cvt_pk_bf16_f32 v89, v114, v115\n"
      "v_mul_f32 v244, s66, v116\n"
      "v_mul_f32 v245, s66, v117\n"
      "s_waitcnt lgkmcnt(4)\n"
      "v_mfma_f32_32x32x16_bf16 v[96:111], a[44:47], a[204:207], v[96:111]\n"
      "ds_read_b64_tr_b16 a[216:217], v7 offset:35840\n"
      "ds_read_b64_tr_b16 a[218:219], v7 offset:36352\n"
      "ds_read_b64_tr_b16 a[220:221], v7 offset:44032\n"
      "ds_read_b64_tr_b16 a[222:223], v7 offset:44544\n"
      "v_mul_f32 v246, s66, v118\n"
      "v_mul_f32 v247, s66, v119\n"
      "v_add_f32 v116, s68, v116\n"
      "s_waitcnt lgkmcnt(6)\n"
      "v_mfma_f32_32x32x16_bf16 v[96:111], a[48:51], a[208:211], v[96:111]\n"
      "buffer_load_dwordx4 v175, s[40:43], s18 offen offset:3072 lds\n"
      "v_add_f32 v117, s68, v117\n"
      "v_add_f32 v118, s68, v118\n"
      "v_add_f32 v119, s68, v119\n"
      "s_waitcnt lgkmcnt(4)\n"
      "v_mfma_f32_32x32x16_bf16 v[96:111], a[52:55], a[212:215], v[96:111]\n"
      "s_waitcnt vmcnt(16) lgkmcnt(0)\n"
      "s_barrier\n"
      "ds_read_b64_tr_b16 a[192:193], v7 offset:49152\n"
      "ds_read_b64_tr_b16 a[194:195], v7 offset:49664\n"
      "s_add_u32 m0, s15, 0x0\n"
      "v_add_u32 v3, 0xffedf800, v3\n"
      "buffer_load_dwordx4 v3, s[28:31], 0 offen offset:0 lds\n"
      "v_exp_f32_e64 v244, v244 clamp\n"
      "v_exp_f32_e64 v245, v245 clamp\n"
      "v_exp_f32_e64 v246, v246 clamp\n"
      "v_mfma_f32_32x32x16_bf16 v[96:111], a[56:59], a[216:219], v[96:111]\n"
      "ds_read_b64_tr_b16 a[196:197], v7 offset:57344\n"
      "ds_read_b64_tr_b16 a[198:199], v7 offset:57856\n"
      "ds_read_b64_tr_b16 a[200:201], v7 offset:50176\n"
      "ds_read_b64_tr_b16 a[202:203], v7 offset:50688\n"
      "v_exp_f32_e64 v247, v247 clamp\n"
      "v_max_f32 v116, s68, v116\n"
      "v_max_f32 v117, s68, v117\n"
      "buffer_load_dwordx4 v3, s[28:31], s19 offen offset:1024 lds\n"
      "v_mfma_f32_32x32x16_bf16 v[96:111], a[60:63], a[220:223], v[96:111]\n"
      "ds_read_b64_tr_b16 a[204:205], v7 offset:58368\n"
      "ds_read_b64_tr_b16 a[206:207], v7 offset:58880\n"
      "v_max_f32 v118, s68, v118\n"
      "v_max_f32 v119, s68, v119\n"
      "v_add_f32 v116, v116, v244\n"
      "s_waitcnt lgkmcnt(6)\n"
      "v_mfma_f32_32x32x16_bf16 v[96:111], a[64:67], a[192:195], v[96:111]\n"
      "buffer_load_dwordx4 v3, s[28:31], s20 offen offset:2048 lds\n"
      "v_add_f32 v117, v117, v245\n"
      "v_add_f32 v118, v118, v246\n"
      "s_mov_b64 exec, 0xffff\n"
      "buffer_load_dwordx2 v[24:25], v22, s[56:59], 0 offen sc1\n"
      "s_mov_b64 exec, -1\n"
      "s_waitcnt lgkmcnt(0)\n"
      "v_mfma_f32_32x32x16_bf16 v[96:111], a[68:71], a[196:199], v[96:111]\n"
      "ds_read_b64_tr_b16 a[208:209], v7 offset:51200\n"
      "ds_read_b64_tr_b16 a[210:211], v7 offset:51712\n"
      "ds_read_b64_tr_b16 a[212:213], v7 offset:59392\n"
      "ds_read_b64_tr_b16 a[214:215], v7 offset:59904\n"
      "v_add_f32 v119, v119, v247\n"
      "v_cvt_pk_bf16_f32 v90, v116, v117\n"
      "v_cvt_pk_bf16_f32 v91, v118, v119\n"
      "v_mfma_f32_32x32x16_bf16 v[96:111], a[72:75], a[200:203], v[96:111]\n"
      "buffer_load_dwordx4 v3, s[28:31], s12 offen offset:3072 lds\n"
      "v_mul_f32 v244, s66, v120\n"
      "v_mul_f32 v245, s66, v121\n"
      "v_mul_f32 v246, s66, v122\n"
      "v_mfma_f32_32x32x16_bf16 v[96:111], a[76:79], a[204:207], v[96:111]\n"
      "ds_read_b64_tr_b16 a[216:217], v7 offset:52224\n"
      "ds_read_b64_tr_b16 a[218:219], v7 offset:52736\n"
      "ds_read_b64_tr_b16 a[220:221], v7 offset:60416\n"
      "ds_read_b64_tr_b16 a[222:223], v7 offset:60928\n"
      "v_mul_f32 v247, s66, v123\n"
      "v_add_f32 v120, s68, v120\n"
      "v_add_f32 v121, s68, v121\n"
      "s_waitcnt lgkmcnt(6)\n"
      "v_mfma_f32_32x32x16_bf16 v[96:111], a[80:83], a[208:211], v[96:111]\n"
      "s_add_u32 m0, s15, 0x8000\n"
      "v_add_u32 v3, 0x60000, v3\n"
      "buffer_load_dwordx4 v3, s[28:31], 0 offen offset:0 lds\n"
      "v_add_f32 v122, s68, v122\n"
      "v_add_f32 v123, s68, v123\n"
      "v_exp_f32_e64 v244, v244 clamp\n"
      "s_waitcnt lgkmcnt(4)\n"
      "v_mfma_f32_32x32x16_bf16 v[96:111], a[84:87], a[212:215], v[96:111]\n"
      "ds_read_b64_tr_b16 a[192:193], v6 offset:16384\n"
      "ds_read_b64_tr_b16 a[194:195], v6 offset:16896\n"
      "ds_read_b64_tr_b16 a[196:197], v6 offset:24576\n"
      "ds_read_b64_tr_b16 a[198:199], v6 offset:25088\n"
      "v_exp_f32_e64 v245, v245 clamp\n"
      "v_exp_f32_e64 v246, v246 clamp\n"
      "v_exp_f32_e64 v247, v247 clamp\n"
      "buffer_load_dwordx4 v3, s[28:31], s19 offen offset:1024 lds\n"
      "s_waitcnt lgkmcnt(4)\n"
      "v_mfma_f32_32x32x16_bf16 v[96:111], a[88:91], a[216:219], v[96:111]\n"
      "ds_read_b64_tr_b16 a[200:201], v6 offset:17408\n"
      "ds_read_b64_tr_b16 a[202:203], v6 offset:17920\n"
      "v_max_f32 v120, s68, v120\n"
      "v_max_f32 v121, s68, v121\n"
      "v_max_f32 v122, s68, v122\n"
      "v_mfma_f32_32x32x16_bf16 v[96:111], a[92:95], a[220:223], v[96:111]\n"
      "buffer_load_dwordx4 v3, s[28:31], s20 offen offset:2048 lds\n"
      "v_max_f32 v123, s68, v123\n"
      "v_add_f32 v120, v120, v244\n"
      "v_add_f32 v121, v121, v245\n"
      "s_waitcnt lgkmcnt(2)\n"
      "v_mfma_f32_32x32x16_bf16 v[96:111], a[96:99], a[192:195], v[96:111]\n"
      "ds_read_b64_tr_b16 a[204:205], v6 offset:25600\n"
      "ds_read_b64_tr_b16 a[206:207], v6 offset:26112\n"
      "ds_read_b64_tr_b16 a[208:209], v6 offset:18432\n"
      "ds_read_b64_tr_b16 a[210:211], v6 offset:18944\n"
      "v_add_f32 v122, v122, v246\n"
      "v_add_f32 v123, v123, v247\n"
      "v_cvt_pk_bf16_f32 v92, v120, v121\n"
      "v_mfma_f32_32x32x16_bf16 v[96:111], a[100:103], a[196:199], v[96:111]\n"
      "buffer_load_dwordx4 v3, s[28:31], s12 offen offset:3072 lds\n"
      "v_cvt_pk_bf16_f32 v93, v122, v123\n"
      "v_mul_f32 v244, s66, v124\n"
      "v_mul_f32 v245, s66, v125\n"
      "s_waitcnt lgkmcnt(4)\n"
      "v_mfma_f32_32x32x16_bf16 v[96:111], a[104:107], a[200:203], v[96:111]\n"
      "ds_read_b64_tr_b16 a[212:213], v6 offset:26624\n"
      "ds_read_b64_tr_b16 a[214:215], v6 offset:27136\n"
      "ds_read_b64_tr_b16 a[216:217], v6 offset:19456\n"
      "ds_read_b64_tr_b16 a[218:219], v6 offset:19968\n"
      "v_mul_f32 v246, s66, v126\n"
      "v_mul_f32 v247, s66, v127\n"
      "v_add_f32 v124, s68, v124\n"
      "s_waitcnt lgkmcnt(6)\n"
      "v_mfma_f32_32x32x16_bf16 v[96:111], a[108:111], a[204:207], v[96:111]\n"
      "s_add_u32 m0, s15, 0x18000\n"
      "v_add_u32 v3, 0x60000, v3\n"
      "buffer_load_dwordx4 v3, s[28:31], 0 offen offset:0 lds\n"
      "v_add_f32 v125, s68, v125\n"
      "v_add_f32 v126, s68, v126\n"
      "v_add_f32 v127, s68, v127\n"
      "s_waitcnt lgkmcnt(4)\n"
      "v_mfma_f32_32x32x16_bf16 v[96:111], a[112:115], a[208:211], v[96:111]\n"
      "ds_read_b64_tr_b16 a[220:221], v6 offset:27648\n"
      "ds_read_b64_tr_b16 a[222:223], v6 offset:28160\n"
      "v_exp_f32_e64 v244, v244 clamp\n"
      "v_exp_f32_e64 v245, v245 clamp\n"
      "v_exp_f32_e64 v246, v246 clamp\n"
      "buffer_load_dwordx4 v3, s[28:31], s19 offen offset:1024 lds\n"
      "buffer_load_dwordx4 v3, s[28:31], s20 offen offset:2048 lds\n"
      "buffer_load_dwordx4 v3, s[28:31], s12 offen offset:3072 lds\n"
      "s_mov_b64 exec, 0xffff\n"
      "s_waitcnt vmcnt(9)\n"
      "v_cmp_eq_u64 vcc, s[60:61], v[24:25]\n"
      "s_nop 0\n"
      "s_cmp_eq_u64 vcc, exec\n"
      "s_cbranch_scc0 L_slow_h\n"
      "L_ok_h:\n"
      "s_mov_b64 exec, -1\n"
      "s_waitcnt lgkmcnt(0)\n"
      "v_mfma_f32_32x32x16_bf16 v[96:111], a[116:119], a[212:215], v[96:111]\n"
      "s_add_u32 s78, s73, 0x400000\n"
      "buffer_load_dwordx4 a[128:131], v1, s[56:59], s78 offen offset:0 sc1\n"
      "s_barrier\n"
      "ds_read_b64_tr_b16 a[192:193], v9 offset:49152\n"
      "ds_read_b64_tr_b16 a[194:195], v9 offset:53248\n"
      "v_exp_f32_e64 v247, v247 clamp\n"
      "v_max_f32 v124, s68, v124\n"
      "v_max_f32 v125, s68, v125\n"
      "v_mfma_f32_32x32x16_bf16 v[96:111], a[120:123], a[216:219], v[96:111]\n"
      "buffer_load_dwordx4 a[132:135], v1, s[56:59], s78 offen offset:1024 sc1\n"
      "ds_read_b64_tr_b16 a[196:197], v12 offset:49152\n"
      "ds_read_b64_tr_b16 a[198:199], v12 offset:53248\n"
      "v_max_f32 v126, s68, v126\n"
      "v_max_f32 v127, s68, v127\n"
      "v_add_f32 v124, v124, v244\n"
      "v_mfma_f32_32x32x16_bf16 v[96:111], a[124:127], a[220:223], v[96:111]\n"
      "ds_read_b64_tr_b16 a[200:201], v9 offset:50176\n"
      "ds_read_b64_tr_b16 a[202:203], v9 offset:54272\n"
      "ds_read_b64_tr_b16 a[204:205], v12 offset:50176\n"
      "ds_read_b64_tr_b16 a[206:207], v12 offset:54272\n"
      "v_add_f32 v125, v125, v245\n"
      "v_add_f32 v126, v126, v246\n"
      "v_add_f32 v127, v127, v247\n"
      "v_cvt_pk_bf16_f32 v94, v124, v125\n"
      "v_cvt_pk_bf16_f32 v95, v126, v127\n"
      "buffer_load_dwordx4 a[136:139], v1, s[56:59], s78 offen offset:2048 sc1\n"
      "ds_read_b64_tr_b16 a[208:209], v9 offset:51200\n"
      "ds_read_b64_tr_b16 a[210:211], v9 offset:55296\n"
      "ds_read_b64_tr_b16 a[212:213], v12 offset:51200\n"
      "ds_read_b64_tr_b16 a[214:215], v12 offset:55296\n"
      "v_cvt_pk_bf16_f32 v224, v96, v97\n"
      "v_cvt_pk_bf16_f32 v225, v98, v99\n"
      "v_cvt_pk_bf16_f32 v226, v100, v101\n"
      "v_cvt_pk_bf16_f32 v227, v102, v103\n"
      "v_cvt_pk_bf16_f32 v228, v104, v105\n"
      "v_cvt_pk_bf16_f32 v229, v106, v107\n"
      "v_cvt_pk_bf16_f32 v230, v108, v109\n"
      "v_cvt_pk_bf16_f32 v231, v110, v111\n"
      "v_dot2_f32_bf16 v242, v88, s64, 0\n"
      "buffer_load_dwordx4 a[140:143], v1, s[56:59], s78 offen offset:3072 sc1\n"
      "v_dot2_f32_bf16 v242, v89, s64, v242\n"
      "v_dot2_f32_bf16 v242, v90, s64, v242\n"
      "v_dot2_f32_bf16 v242, v91, s64, v242\n"
      "v_dot2_f32_bf16 v242, v92, s64, v242\n"
      "v_dot2_f32_bf16 v242, v93, s64, v242\n"
      "v_dot2_f32_bf16 v242, v94, s64, v242\n"
      "v_dot2_f32_bf16 v242, v95, s64, v242\n"
      "s_waitcnt lgkmcnt(4)\n"
      "s_add_u32 s78, s73, 0x401000\n"
      "buffer_load_dwordx4 a[144:147], v1, s[56:59], s78 offen offset:0 sc1\n"
      "buffer_load_dwordx4 a[148:151], v1, s[56:59], s78 offen offset:1024 sc1\n"
      "buffer_load_dwordx4 a[152:155], v1, s[56:59], s78 offen offset:2048 sc1\n"
      "buffer_load_dwordx4 a[156:159], v1, s[56:59], s78 offen offset:3072 sc1\n"
      "s_add_u32 s78, s73, 0x402000\n"
      "buffer_load_dwordx4 a[160:163], v1, s[56:59], s78 offen offset:0 sc1\n"
      "buffer_load_dwordx4 a[164:167], v1, s[56:59], s78 offen offset:1024 sc1\n"
      "buffer_load_dwordx4 a[168:171], v1, s[56:59], s78 offen offset:2048 sc1\n"
      "buffer_load_dwordx4 a[172:175], v1, s[56:59], s78 offen offset:3072 sc1\n"
      "s_waitcnt vmcnt(11)\n"
      "v_mfma_f32_32x32x16_bf16 v[32:47], a[192:195], a[128:131], v[32:47]\n"
      "s_add_u32 s78, s73, 0x403000\n"
      "buffer_load_dwordx4 a[176:179], v1, s[56:59], s78 offen offset:0 sc1\n"
      "v_mfma_f32_32x32x16_bf16 v[48:63], a[196:199], a[128:131], 0\n"
      "v_mfma_f32_32x32x16_bf16 v[112:127], v[88:91], v[224:227], 0\n"
      "buffer_load_dwordx4 a[180:183], v1, s[56:59], s78 offen offset:1024 sc1\n"
      "v_mfma_f32_32x32x16_bf16 v[112:127], v[92:95], v[228:231], v[112:127]\n"
      "v_bfe_u32 v241, v0, 4, 1\n"
      "ds_read_b64_tr_b16 a[216:217], v9 offset:52224\n"
      "ds_read_b64_tr_b16 a[218:219], v9 offset:56320\n"
      "v_and_b32 v190, 15, v0\n"
      "v_lshl_add_u32 v187, v172, 4, v190\n"
      "s_waitcnt vmcnt(12)\n"
      "v_mfma_f32_32x32x16_bf16 v[32:47], a[200:203], a[132:135], v[32:47]\n"
      "buffer_load_dwordx4 a[184:187], v1, s[56:59], s78 offen offset:2048 sc1\n"
      "v_mfma_f32_32x32x16_bf16 v[48:63], a[204:207], a[132:135], v[48:63]\n"
      "ds_read_b64_tr_b16 a[220:221], v12 offset:52224\n"
      "ds_read_b64_tr_b16 a[222:223], v12 offset:56320\n"
      "v_lshlrev_b32 v186, 2, v187\n"
      "v_lshl_add_u32 v186, v241, 13, v186\n"
      "v_lshlrev_b32 v200, 4, v187\n"
      "s_waitcnt vmcnt(12) lgkmcnt(4)\n"
      "v_mfma_f32_32x32x16_bf16 v[32:47], a[208:211], a[136:139], v[32:47]\n"
      "buffer_load_dwordx4 a[188:191], v1, s[56:59], s78 offen offset:3072 sc1\n"
      "v_lshl_add_u32 v187, v241, 16, v200\n"
      "s_lshl_b32 s22, s7, 7\n"
      "v_mfma_f32_32x32x16_bf16 v[48:63], a[212:215], a[136:139], v[48:63]\n"
      "ds_read_b64_tr_b16 a[192:193], v11 offset:0\n"
      "ds_read_b64_tr_b16 a[194:195], v11 offset:4096\n"
      "ds_read_b64_tr_b16 a[196:197], v14 offset:0\n"
      "ds_read_b64_tr_b16 a[198:199], v14 offset:4096\n"
      "s_add_u32 s22, s22, s6\n"
      "s_lshl_b32 s88, s22, 7\n"
      "s_add_u32 s88, s88, 0x100000\n"
      "s_waitcnt vmcnt(12) lgkmcnt(4)\n"
      "v_mfma_f32_32x32x16_bf16 v[32:47], a[216:219], a[140:143], v[32:47]\n"
      "s_add_u32 m0, s15, 0x1c000\n"
      "v_add_u32 v3, 0x60000, v3\n"
      "buffer_load_dwordx4 v3, s[28:31], 0 offen offset:0 lds\n"
      "s_lshl_b32 s22, s22, 10\n"
      "s_add_u32 s22, s22, 0x200000\n"
      "v_add_u32 v187, s22, v187\n"
      "v_mfma_f32_32x32x16_bf16 v[48:63], a[220:223], a[140:143], v[48:63]\n"
      "ds_read_b64_tr_b16 a[200:201], v11 offset:1024\n"
      "ds_read_b64_tr_b16 a[202:203], v11 offset:5120\n"
      "ds_read_b64_tr_b16 a[204:205], v14 offset:1024\n"
      "ds_read_b64_tr_b16 a[206:207], v14 offset:5120\n"
      "v_lshrrev_b32 v241, 4, v0\n"
      "v_lshl_add_u32 v190, v190, 2, v241\n"
      "s_waitcnt vmcnt(12) lgkmcnt(6)\n"
      "v_mfma_f32_32x32x16_bf16 v[32:47], a[192:195], a[144:147], v[32:47]\n"
      "buffer_load_dwordx4 v3, s[28:31], s19 offen offset:1024 lds\n"
      "v_lshlrev_b32 v190, 2, v190\n"
      "s_add_u32 s23, s15, 0x24000\n"
      "v_add_u32 v188, s23, v1\n"
      "s_waitcnt lgkmcnt(4)\n"
      "v_mfma_f32_32x32x16_bf16 v[48:63], a[196:199], a[144:147], v[48:63]\n"
      "ds_read_b64_tr_b16 a[208:209], v11 offset:2048\n"
      "ds_read_b64_tr_b16 a[210:211], v11 offset:6144\n"
      "ds_read_b64_tr_b16 a[212:213], v14 offset:2048\n"
      "ds_read_b64_tr_b16 a[214:215], v14 offset:6144\n"
      "v_xor_b32 v189, 0x2000, v188\n"
      "v_cndmask_b32 v232, v112, v120, s[70:71]\n"
      "s_waitcnt vmcnt(12) lgkmcnt(6)\n"
      "v_mfma_f32_32x32x16_bf16 v[32:47], a[200:203], a[148:151], v[32:47]\n"
      "buffer_load_dwordx4 v3, s[28:31], s20 offen offset:2048 lds\n"
      "v_cndmask_b32 v233, v113, v121, s[70:71]\n"
      "v_cndmask_b32 v234, v114, v122, s[70:71]\n"
      "v_cndmask_b32 v235, v115, v123, s[70:71]\n"
      "s_waitcnt lgkmcnt(4)\n"
      "v_mfma_f32_32x32x16_bf16 v[48:63], a[204:207], a[148:151], v[48:63]\n"
      "ds_read_b64_tr_b16 a[216:217], v11 offset:3072\n"
      "ds_read_b64_tr_b16 a[218:219], v11 offset:7168\n"
      "ds_read_b64_tr_b16 a[220:221], v14 offset:3072\n"
      "ds_read_b64_tr_b16 a[222:223], v14 offset:7168\n"
      "v_cndmask_b32 v236, v116, v124, s[70:71]\n"
      "v_cndmask_b32 v237, v117, v125, s[70:71]\n"
      "v_cndmask_b32 v238, v118, v126, s[70:71]\n"
      "v_cndmask_b32 v239, v119, v127, s[70:71]\n"
      "s_waitcnt vmcnt(12) lgkmcnt(6)\n"
      "v_mfma_f32_32x32x16_bf16 v[32:47], a[208:211], a[152:155], v[32:47]\n"
      "buffer_store_dwordx4 v[232:235], v187, s[56:59], 0 offen offset:0 sc1\n"
      "s_waitcnt lgkmcnt(4)\n"
      "v_mfma_f32_32x32x16_bf16 v[48:63], a[212:215], a[152:155], v[48:63]\n"
      "s_waitcnt lgkmcnt(0)\n"
      "s_barrier\n"
      "ds_read_b64_tr_b16 a[192:193], v10 offset:0\n"
      "ds_read_b64_tr_b16 a[194:195], v10 offset:4096\n"
      "ds_read_b64_tr_b16 a[196:197], v13 offset:0\n"
      "ds_read_b64_tr_b16 a[198:199], v13 offset:4096\n"
      "buffer_store_dwordx4 v[236:239], v187, s[56:59], 0 offen offset:512 sc1\n"
      "s_waitcnt vmcnt(13)\n"
      "v_mfma_f32_32x32x16_bf16 v[32:47], a[216:219], a[156:159], v[32:47]\n"
      "ds_read_b64_tr_b16 a[200:201], v10 offset:1024\n"
      "ds_read_b64_tr_b16 a[202:203], v10 offset:5120\n"
      "ds_read_b64_tr_b16 a[204:205], v13 offset:1024\n"
      "ds_read_b64_tr_b16 a[206:207], v13 offset:5120\n"
      "v_mfma_f32_32x32x16_bf16 v[48:63], a[220:223], a[156:159], v[48:63]\n"
      "buffer_store_dword v242, v186, s[56:59], s88 offen sc1\n"
      "s_waitcnt vmcnt(13) lgkmcnt(6)\n"
      "v_mfma_f32_32x32x16_bf16 v[32:47], a[192:195], a[160:163], v[32:47]\n"
      "ds_read_b64_tr_b16 a[208:209], v10 offset:2048\n"
      "ds_read_b64_tr_b16 a[210:211], v10 offset:6144\n"
      "ds_read_b64_tr_b16 a[212:213], v13 offset:2048\n"
      "ds_read_b64_tr_b16 a[214:215], v13 offset:6144\n"
      "s_waitcnt lgkmcnt(8)\n"
      "v_mfma_f32_32x32x16_bf16 v[48:63], a[196:199], a[160:163], v[48:63]\n"
      "buffer_load_dwordx4 v3, s[28:31], s12 offen offset:3072 lds\n"
      "s_waitcnt vmcnt(13) lgkmcnt(4)\n"
      "v_mfma_f32_32x32x16_bf16 v[32:47], a[200:203], a[164:167], v[32:47]\n"
      "ds_read_b64_tr_b16 a[216:217], v10 offset:3072\n"
      "ds_read_b64_tr_b16 a[218:219], v10 offset:7168\n"
      "ds_read_b64_tr_b16 a[220:221], v13 offset:3072\n"
      "ds_read_b64_tr_b16 a[222:223], v13 offset:7168\n"
      "v_mfma_f32_32x32x16_bf16 v[48:63], a[204:207], a[164:167], v[48:63]\n"
      "s_add_u32 m0, s15, 0x4000\n"
      "s_nop 0\n"
      "buffer_load_dwordx4 v4, s[44:47], 0 offen offset:0 lds\n"
      "s_waitcnt vmcnt(13) lgkmcnt(4)\n"
      "v_mfma_f32_32x32x16_bf16 v[32:47], a[208:211], a[168:171], v[32:47]\n"
      "ds_read_b64_tr_b16 a[192:193], v10 offset:16384\n"
      "ds_read_b64_tr_b16 a[194:195], v10 offset:20480\n"
      "ds_read_b64_tr_b16 a[196:197], v13 offset:16384\n"
      "ds_read_b64_tr_b16 a[198:199], v13 offset:20480\n"
      "v_mfma_f32_32x32x16_bf16 v[48:63], a[212:215], a[168:171], v[48:63]\n"
      "buffer_load_dwordx4 v4, s[44:47], s13 offen offset:1024 lds\n"
      "s_waitcnt vmcnt(13) lgkmcnt(4)\n"
      "v_mfma_f32_32x32x16_bf16 v[32:47], a[216:219], a[172:175], v[32:47]\n"
      "ds_read_b64_tr_b16 a[200:201], v10 offset:17408\n"
      "ds_read_b64_tr_b16 a[202:203], v10 offset:21504\n"
      "ds_read_b64_tr_b16 a[204:205], v13 offset:17408\n"
      "ds_read_b64_tr_b16 a[206:207], v13 offset:21504\n"
      "v_mfma_f32_32x32x16_bf16 v[48:63], a[220:223], a[172:175], v[48:63]\n"
      "buffer_load_dwordx4 v4, s[44:47], s14 offen offset:2048 lds\n"
      "s_waitcnt vmcnt(13) lgkmcnt(4)\n"
      "v_mfma_f32_32x32x16_bf16 v[32:47], a[192:195], a[176:179], v[32:47]\n"
      "ds_read_b64_tr_b16 a[208:209], v10 offset:18432\n"
      "ds_read_b64_tr_b16 a[210:211], v10 offset:22528\n"
      "ds_read_b64_tr_b16 a[212:213], v13 offset:18432\n"
      "ds_read_b64_tr_b16 a[214:215], v13 offset:22528\n"
      "v_mfma_f32_32x32x16_bf16 v[48:63], a[196:199], a[176:179], v[48:63]\n"
      "buffer_load_dwordx4 v4, s[44:47], s2 offen offset:3072 lds\n"
      "s_waitcnt vmcnt(13) lgkmcnt(4)\n"
      "v_mfma_f32_32x32x16_bf16 v[32:47], a[200:203], a[180:183], v[32:47]\n"
      "ds_read_b64_tr_b16 a[216:217], v10 offset:19456\n"
      "ds_read_b64_tr_b16 a[218:219], v10 offset:23552\n"
      "ds_read_b64_tr_b16 a[220:221], v13 offset:19456\n"
      "ds_read_b64_tr_b16 a[222:223], v13 offset:23552\n"
      "v_mfma_f32_32x32x16_bf16 v[48:63], a[204:207], a[180:183], v[48:63]\n"
      "s_add_u32 m0, s15, 0xc000\n"
      "v_add_u32 v4, 0x70000, v4\n"
      "buffer_load_dwordx4 v4, s[44:47], 0 offen offset:0 lds\n"
      "s_waitcnt vmcnt(13) lgkmcnt(4)\n"
      "v_mfma_f32_32x32x16_bf16 v[32:47], a[208:211], a[184:187], v[32:47]\n"
      "v_mfma_f32_32x32x16_bf16 v[48:63], a[212:215], a[184:187], v[48:63]\n"
      "s_waitcnt vmcnt(12) lgkmcnt(2)\n"
      "v_mfma_f32_32x32x16_bf16 v[32:47], a[216:219], a[188:191], v[32:47]\n"
      "buffer_load_dwordx4 v4, s[44:47], s13 offen offset:1024 lds\n"
      "s_waitcnt lgkmcnt(0)\n"
      "v_mfma_f32_32x32x16_bf16 v[48:63], a[220:223], a[188:191], v[48:63]\n"
      "s_nop 11\n"
      "ds_write_b128 v188, v[48:51] offset:0\n"
      "ds_write_b128 v188, v[52:55] offset:1024\n"
      "buffer_load_dwordx4 v4, s[44:47], s14 offen offset:2048 lds\n"
      "ds_write_b128 v188, v[56:59] offset:2048\n"
      "ds_write_b128 v188, v[60:63] offset:3072\n"
      "buffer_load_dwordx4 v4, s[44:47], s2 offen offset:3072 lds\n"
      "s_waitcnt lgkmcnt(0)\n"
      "s_barrier\n"
      "ds_read_b128 v[144:147], v189 offset:0\n"
      "ds_read_b128 v[148:151], v189 offset:1024\n"
      "ds_read_b128 v[152:155], v189 offset:2048\n"
      "ds_read_b128 v[156:159], v189 offset:3072\n"
      "s_add_u32 m0, s15, 0x20000\n"
      "v_add_u32 v4, 0x70000, v4\n"
      "buffer_load_dwordx4 v4, s[44:47], 0 offen offset:0 lds\n"
      "s_xor_b32 s22, s74, 0x600000\n"
      "s_waitcnt lgkmcnt(3)\n"
      "v_pk_add_f32 v[144:145], v[144:145], v[32:33]\n"
      "v_pk_add_f32 v[146:147], v[146:147], v[34:35]\n"
      "s_waitcnt lgkmcnt(2)\n"
      "v_pk_add_f32 v[148:149], v[148:149], v[36:37]\n"
      "v_pk_add_f32 v[150:151], v[150:151], v[38:39]\n"
      "v_cvt_pk_bf16_f32 v224, v144, v145\n"
      "v_cvt_pk_bf16_f32 v225, v146, v147\n"
      "v_cvt_pk_bf16_f32 v226, v148, v149\n"
      "v_cvt_pk_bf16_f32 v227, v150, v151\n"
      "buffer_store_dwordx4 v[224:227], v174, s[56:59], s22 offen offset:0 sc1\n"
      "s_waitcnt lgkmcnt(1)\n"
      "v_pk_add_f32 v[152:153], v[152:153], v[40:41]\n"
      "v_pk_add_f32 v[154:155], v[154:155], v[42:43]\n"
      "s_waitcnt lgkmcnt(0)\n"
      "v_pk_add_f32 v[156:157], v[156:157], v[44:45]\n"
      "v_pk_add_f32 v[158:159], v[158:159], v[46:47]\n"
      "v_cvt_pk_bf16_f32 v228, v152, v153\n"
      "v_cvt_pk_bf16_f32 v229, v154, v155\n"
      "v_cvt_pk_bf16_f32 v230, v156, v157\n"
      "v_cvt_pk_bf16_f32 v231, v158, v159\n"
      "buffer_store_dwordx4 v[228:231], v174, s[56:59], s22 offen offset:1024 sc1\n"
      "v_dot2_f32_bf16 v232, v224, s64, 0\n"
      "v_dot2_f32_bf16 v233, v224, v224, 0\n"
      "v_dot2_f32_bf16 v232, v225, s64, v232\n"
      "v_dot2_f32_bf16 v233, v225, v225, v233\n"
      "v_dot2_f32_bf16 v232, v226, s64, v232\n"
      "v_dot2_f32_bf16 v233, v226, v226, v233\n"
      "v_dot2_f32_bf16 v232, v227, s64, v232\n"
      "v_dot2_f32_bf16 v233, v227, v227, v233\n"
      "v_dot2_f32_bf16 v232, v228, s64, v232\n"
      "v_dot2_f32_bf16 v233, v228, v228, v233\n"
      "v_dot2_f32_bf16 v232, v229, s64, v232\n"
      "v_dot2_f32_bf16 v233, v229, v229, v233\n"
      "v_dot2_f32_bf16 v232, v230, s64, v232\n"
      "buffer_load_dwordx4 v4, s[44:47], s13 offen offset:1024 lds\n"
      "v_dot2_f32_bf16 v233, v230, v230, v233\n"
      "v_dot2_f32_bf16 v232, v231, s64, v232\n"
      "v_dot2_f32_bf16 v233, v231, v231, v233\n"
      "ds_read_b64_tr_b16 a[192:193], v6 offset:0\n"
      "ds_read_b64_tr_b16 a[194:195], v6 offset:512\n"
      "ds_read_b64_tr_b16 a[196:197], v6 offset:8192\n"
      "ds_read_b64_tr_b16 a[198:199], v6 offset:8704\n"
      "ds_read_b64_tr_b16 a[200:201], v6 offset:1024\n"
      "ds_read_b64_tr_b16 a[202:203], v6 offset:1536\n"
      "ds_read_b64_tr_b16 a[204:205], v6 offset:9216\n"
      "ds_read_b64_tr_b16 a[206:207], v6 offset:9728\n"
      "ds_read_b64_tr_b16 a[208:209], v6 offset:2048\n"
      "ds_read_b64_tr_b16 a[210:211], v6 offset:2560\n"
      "buffer_load_dwordx4 v4, s[44:47], s14 offen offset:2048 lds\n"
      "v_permlane32_swap_b32 v232, v233\n"
      "v_add_f32 v232, v232, v233\n"
      "buffer_store_dword v232, v190, s[56:59], s89 offen sc1\n"
      "s_waitcnt vmcnt(15)\n"
      "s_mov_b64 exec, 1\n"
      "s_xor_b32 s86, s79, 0x80000\n"
      "buffer_store_dwordx2 v[26:27], v1, s[56:59], s86 offen sc1\n"
      "s_mov_b64 exec, -1\n"
      "ds_read_b64_tr_b16 a[212:213], v6 offset:10240\n"
      "ds_read_b64_tr_b16 a[214:215], v6 offset:10752\n"
      "s_waitcnt lgkmcnt(2)\n"
      "v_mfma_f32_32x32x16_bf16 v[128:143], a[192:195], a[0:3], 0\n"
      "s_lshl_b32 s22, s10, 6\n"
      "v_xor_b32 v191, s22, v9\n"
      "v_xor_b32 v192, s22, v10\n"
      "v_xor_b32 v193, s22, v11\n"
      "v_lshrrev_b32 v196, 2, v0\n"
      "v_mfma_f32_32x32x16_bf16 v[128:143], a[196:199], a[4:7], v[128:143]\n"
      "buffer_load_dwordx4 v4, s[44:47], s2 offen offset:3072 lds\n"
      "v_mfma_f32_32x32x16_bf16 v[128:143], a[200:203], a[8:11], v[128:143]\n"
      "ds_read_b64_tr_b16 a[216:217], v6 offset:3072\n"
      "ds_read_b64_tr_b16 a[218:219], v6 offset:3584\n"
      "ds_read_b64_tr_b16 a[220:221], v6 offset:11264\n"
      "ds_read_b64_tr_b16 a[222:223], v6 offset:11776\n"
      "v_xor_b32 v196, v196, v0\n"
      "v_mfma_f32_32x32x16_bf16 v[128:143], a[204:207], a[12:15], v[128:143]\n"
      "s_add_u32 m0, s15, 0x10000\n"
      "v_add_u32 v4, 0x70000, v4\n"
      "buffer_load_dwordx4 v4, s[44:47], 0 offen offset:0 lds\n"
      "v_mfma_f32_32x32x16_bf16 v[128:143], a[208:211], a[16:19], v[128:143]\n"
      "ds_read_b64_tr_b16 a[192:193], v6 offset:32768\n"
      "ds_read_b64_tr_b16 a[194:195], v6 offset:33280\n"
      "ds_read_b64_tr_b16 a[196:197], v6 offset:40960\n"
      "ds_read_b64_tr_b16 a[198:199], v6 offset:41472\n"
      "v_bfe_u32 v196, v196, 2, 1\n"
      "s_waitcnt lgkmcnt(8)\n"
      "v_mfma_f32_32x32x16_bf16 v[128:143], a[212:215], a[20:23], v[128:143]\n"
      "buffer_load_dwordx4 v4, s[44:47], s13 offen offset:1024 lds\n"
      "s_waitcnt lgkmcnt(4)\n"
      "v_mfma_f32_32x32x16_bf16 v[128:143], a[216:219], a[24:27], v[128:143]\n"
      "ds_read_b64_tr_b16 a[200:201], v6 offset:33792\n"
      "ds_read_b64_tr_b16 a[202:203], v6 offset:34304\n"
      "v_add_u32 v196, -1, v196\n"
      "ds_read_b64_tr_b16 a[204:205], v6 offset:41984\n"
      "ds_read_b64_tr_b16 a[206:207], v6 offset:42496\n"
      "v_mfma_f32_32x32x16_bf16 v[128:143], a[220:223], a[28:31], v[128:143]\n"
      "buffer_load_dwordx4 v4, s[44:47], s14 offen offset:2048 lds\n"
      "v_and_b32 v196, s64, v196\n"
      "s_waitcnt lgkmcnt(4)\n"
      "v_mfma_f32_32x32x16_bf16 v[128:143], a[192:195], a[32:35], v[128:143]\n"
      "ds_read_b64_tr_b16 a[208:209], v6 offset:34816\n"
      "ds_read_b64_tr_b16 a[210:211], v6 offset:35328\n"
      "v_mov_b32 v197, v196\n"
      "v_mov_b32 v198, v196\n"
      "v_mov_b32 v199, v196\n"
      "v_mfma_f32_32x32x16_bf16 v[128:143], a[196:199], a[36:39], v[128:143]\n"
      "buffer_load_dwordx4 v4, s[44:47], s2 offen offset:3072 lds\n"
      "s_waitcnt lgkmcnt(2)\n"
      "v_mfma_f32_32x32x16_bf16 v[128:143], a[200:203], a[40:43], v[128:143]\n"
      "ds_read_b64_tr_b16 a[212:213], v6 offset:43008\n"
      "ds_read_b64_tr_b16 a[214:215], v6 offset:43520\n"
      "ds_read_b64_tr_b16 a[216:217], v6 offset:35840\n"
      "ds_read_b64_tr_b16 a[218:219], v6 offset:36352\n"
      "v_add_u32 v16, 0x10000, v15\n"
      "v_add_u32 v17, 0x20000, v15\n"
      "v_mfma_f32_32x32x16_bf16 v[128:143], a[204:207], a[44:47], v[128:143]\n"
      "s_add_u32 m0, s15, 0x14000\n"
      "v_add_u32 v4, 0xffeb0080, v4\n"
      "buffer_load_dwordx4 v4, s[44:47], 0 offen offset:0 lds\n"
      "v_add_u32 v19, 0x10000, v18\n"
      "s_waitcnt lgkmcnt(4)\n"
      "v_mfma_f32_32x32x16_bf16 v[128:143], a[208:211], a[48:51], v[128:143]\n"
      "ds_read_b64_tr_b16 a[220:221], v6 offset:44032\n"
      "ds_read_b64_tr_b16 a[222:223], v6 offset:44544\n"
      "v_add_u32 v20, 0x20000, v18\n"
      "v_lshlrev_b32 v194, 2, v171\n"
      "v_add_u32 v194, 0x24000, v194\n"
      "s_waitcnt lgkmcnt(4)\n"
      "v_mfma_f32_32x32x16_bf16 v[128:143], a[212:215], a[52:55], v[128:143]\n"
      "s_waitcnt vmcnt(21) lgkmcnt(0)\n"
      "s_barrier\n"
      "ds_read_b64_tr_b16 a[192:193], v7 offset:32768\n"
      "ds_read_b64_tr_b16 a[194:195], v7 offset:33280\n"
      "buffer_load_dwordx4 v4, s[44:47], s13 offen offset:1024 lds\n"
      "v_mfma_f32_32x32x16_bf16 v[128:143], a[216:219], a[56:59], v[128:143]\n"
      "ds_read_b64_tr_b16 a[196:197], v7 offset:40960\n"
      "ds_read_b64_tr_b16 a[198:199], v7 offset:41472\n"
      "ds_read_b64_tr_b16 a[200:201], v7 offset:33792\n"
      "ds_read_b64_tr_b16 a[202:203], v7 offset:34304\n"
      "s_xor_b32 s22, s10, s11\n"
      "s_lshl_b32 s23, s11, 1\n"
      "v_mfma_f32_32x32x16_bf16 v[128:143], a[220:223], a[60:63], v[128:143]\n"
      "buffer_load_dwordx4 v4, s[44:47], s14 offen offset:2048 lds\n"
      "s_add_u32 s22, s22, s23\n"
      "s_waitcnt lgkmcnt(4)\n"
      "v_mfma_f32_32x32x16_bf16 v[128:143], a[192:195], a[64:67], v[128:143]\n"
      "s_waitcnt vmcnt(9)\n"
      "s_mov_b64 exec, 1\n"
      "s_xor_b32 s86, s79, 0x40000\n"
      "buffer_store_dwordx2 v[26:27], v1, s[56:59], s86 offen sc1\n"
      "s_mov_b64 exec, -1\n"
      "ds_read_b64_tr_b16 a[204:205], v7 offset:41984\n"
      "ds_read_b64_tr_b16 a[206:207], v7 offset:42496\n"
      "ds_read_b64_tr_b16 a[208:209], v7 offset:34816\n"
      "ds_read_b64_tr_b16 a[210:211], v7 offset:35328\n"
      "s_lshl_b32 s22, s22, 7\n"
      "v_add_u32 v195, s22, v194\n"
      "s_waitcnt lgkmcnt(4)\n"
      "v_mfma_f32_32x32x16_bf16 v[128:143], a[196:199], a[68:71], v[128:143]\n"
      "buffer_load_dwordx4 v4, s[44:47], s2 offen offset:3072 lds\n"
      "s_lshl_b32 s22, s7, 10\n"
      "v_mfma_f32_32x32x16_bf16 v[128:143], a[200:203], a[72:75], v[128:143]\n"
      "ds_read_b64_tr_b16 a[212:213], v7 offset:43008\n"
      "ds_read_b64_tr_b16 a[214:215], v7 offset:43520\n"
      "ds_read_b64_tr_b16 a[216:217], v7 offset:35840\n"
      "ds_read_b64_tr_b16 a[218:219], v7 offset:36352\n"
      "s_add_u32 s22, s22, 0x4000\n"
      "v_add_u32 v200, s22, v200\n"
      "s_waitcnt lgkmcnt(6)\n"
      "v_mfma_f32_32x32x16_bf16 v[128:143], a[204:207], a[76:79], v[128:143]\n"
      "s_add_u32 m0, s15, 0x0\n"
      "v_add_u32 v4, 0x70000, v4\n"
      "buffer_load_dwordx4 v4, s[44:47], 0 offen offset:0 lds\n"
      "v_add_u32 v203, 0x200, v200\n"
      "s_waitcnt lgkmcnt(4)\n"
      "v_mfma_f32_32x32x16_bf16 v[128:143], a[208:211], a[80:83], v[128:143]\n"
      "ds_read_b64_tr_b16 a[220:221], v7 offset:44032\n"
      "ds_read_b64_tr_b16 a[222:223], v7 offset:44544\n"
      "ds_read_b64_tr_b16 a[192:193], v7 offset:49152\n"
      "ds_read_b64_tr_b16 a[194:195], v7 offset:49664\n"
      "s_add_u32 s22, s81, 0x6000\n"
      "v_lshl_add_u32 v240, v172, 4, s22\n"
      "s_waitcnt lgkmcnt(6)\n"
      "v_mfma_f32_32x32x16_bf16 v[128:143], a[212:215], a[84:87], v[128:143]\n"
      "buffer_load_dwordx4 v4, s[44:47], s13 offen offset:1024 lds\n"
      "v_add_u32 v241, 0x80, v240\n"
      "v_cndmask_b32 v200, v200, v240, s[70:71]\n"
      "v_cndmask_b32 v203, v203, v241, s[70:71]\n"
      "v_mul_f32 v202, s66, v185\n"
      "s_waitcnt lgkmcnt(4)\n"
      "v_mfma_f32_32x32x16_bf16 v[128:143], a[216:219], a[88:91], v[128:143]\n"
      "ds_read_b64_tr_b16 a[196:197], v7 offset:57344\n"
      "ds_read_b64_tr_b16 a[198:199], v7 offset:57856\n"
      "ds_read_b64_tr_b16 a[200:201], v7 offset:50176\n"
      "ds_read_b64_tr_b16 a[202:203], v7 offset:50688\n"
      "s_waitcnt lgkmcnt(6)\n"
      "v_mfma_f32_32x32x16_bf16 v[128:143], a[220:223], a[92:95], v[128:143]\n"
      "buffer_load_dwordx4 v4, s[44:47], s14 offen offset:2048 lds\n"
      "s_waitcnt lgkmcnt(4)\n"
      "v_mfma_f32_32x32x16_bf16 v[128:143], a[192:195], a[96:99], v[128:143]\n"
      "ds_read_b64_tr_b16 a[204:205], v7 offset:58368\n"
      "ds_read_b64_tr_b16 a[206:207], v7 offset:58880\n"
      "ds_read_b64_tr_b16 a[208:209], v7 offset:51200\n"
      "ds_read_b64_tr_b16 a[210:211], v7 offset:51712\n"
      "s_waitcnt lgkmcnt(6)\n"
      "v_mfma_f32_32x32x16_bf16 v[128:143], a[196:199], a[100:103], v[128:143]\n"
      "buffer_load_dwordx4 v4, s[44:47], s2 offen offset:3072 lds\n"
      "s_waitcnt lgkmcnt(4)\n"
      "v_mfma_f32_32x32x16_bf16 v[128:143], a[200:203], a[104:107], v[128:143]\n"
      "ds_read_b64_tr_b16 a[212:213], v7 offset:59392\n"
      "ds_read_b64_tr_b16 a[214:215], v7 offset:59904\n"
      "ds_read_b64_tr_b16 a[216:217], v7 offset:52224\n"
      "ds_read_b64_tr_b16 a[218:219], v7 offset:52736\n"
      "s_waitcnt lgkmcnt(6)\n"
      "v_mfma_f32_32x32x16_bf16 v[128:143], a[204:207], a[108:111], v[128:143]\n"
      "s_add_u32 m0, s15, 0x8000\n"
      "v_add_u32 v4, 0x70000, v4\n"
      "buffer_load_dwordx4 v4, s[44:47], 0 offen offset:0 lds\n"
      "s_waitcnt lgkmcnt(4)\n"
      "v_mfma_f32_32x32x16_bf16 v[128:143], a[208:211], a[112:115], v[128:143]\n"
      "ds_read_b64_tr_b16 a[220:221], v7 offset:60416\n"
      "ds_read_b64_tr_b16 a[222:223], v7 offset:60928\n"
      "s_waitcnt lgkmcnt(4)\n"
      "v_mfma_f32_32x32x16_bf16 v[128:143], a[212:215], a[116:119], v[128:143]\n"
      "s_waitcnt lgkmcnt(0)\n"
      "s_barrier\n"
      "ds_read_b64_tr_b16 a[192:193], v191 offset:16384\n"
      "ds_read_b64_tr_b16 a[194:195], v191 offset:17408\n"
      "buffer_load_dwordx4 v4, s[44:47], s13 offen offset:1024 lds\n"
      "v_mfma_f32_32x32x16_bf16 v[128:143], a[216:219], a[120:123], v[128:143]\n"
      "ds_read_b64_tr_b16 a[196:197], v191 offset:18432\n"
      "ds_read_b64_tr_b16 a[198:199], v191 offset:19456\n"
      "ds_read_b64_tr_b16 a[200:201], v191 offset:20480\n"
      "ds_read_b64_tr_b16 a[202:203], v191 offset:21504\n"
      "v_mfma_f32_32x32x16_bf16 v[128:143], a[220:223], a[124:127], v[128:143]\n"
      "buffer_load_dwordx4 v4, s[44:47], s14 offen offset:2048 lds\n"
      "s_cmp_eq_u32 s92, 7\n"
      "s_cbranch_scc1 L_spare\n"
      "s_mul_i32 s21, s93, 14336\n"
      "s_add_u32 s21, s21, s95\n"
      "s_mov_b64 exec, 0x3fff\n"
      "buffer_load_dword a247, v173, s[48:51], s21 offen\n"
      "s_mov_b64 exec, -1\n"
      "s_mul_i32 s21, s93, 4096\n"
      "s_add_u32 s21, s21, s95\n"
      "s_mov_b64 exec, 0xf\n"
      "buffer_load_dword a248, v173, s[32:35], s21 offen\n"
      "s_mov_b64 exec, -1\n"
      "s_waitcnt lgkmcnt(0)\n"
      "v_mfma_f32_16x16x32_bf16 v[144:147], v[196:199], a[192:195], 0\n"
      "ds_read_b64_tr_b16 a[204:205], v191 offset:22528\n"
      "ds_read_b64_tr_b16 a[206:207], v191 offset:23552\n"
      "ds_read_b64_tr_b16 a[208:209], v191 offset:49152\n"
      "ds_read_b64_tr_b16 a[210:211], v191 offset:50176\n"
      "v_mfma_f32_16x16x32_bf16 v[144:147], v[196:199], a[196:199], v[144:147]\n"
      "ds_read_b64_tr_b16 a[212:213], v191 offset:51200\n"
      "ds_read_b64_tr_b16 a[214:215], v191 offset:52224\n"
      "v_mfma_f32_16x16x32_bf16 v[144:147], v[196:199], a[200:203], v[144:147]\n"
      "ds_read_b64_tr_b16 a[216:217], v191 offset:53248\n"
      "ds_read_b64_tr_b16 a[218:219], v191 offset:54272\n"
      "buffer_load_dwordx4 v4, s[44:47], s2 offen offset:3072 lds\n"
      "v_mul_f32 v224, v202, v128\n"
      "v_mul_f32 v225, v202, v129\n"
      "v_mul_f32 v226, v202, v130\n"
      "v_mul_f32 v227, v202, v131\n"
      "s_waitcnt lgkmcnt(6)\n"
      "v_mfma_f32_16x16x32_bf16 v[144:147], v[196:199], a[204:207], v[144:147]\n"
      "ds_read_b64_tr_b16 a[220:221], v191 offset:55296\n"
      "ds_read_b64_tr_b16 a[222:223], v191 offset:56320\n"
      "v_fma_f32 v128, v128, v185, s68\n"
      "v_fma_f32 v129, v129, v185, s68\n"
      "v_fma_f32 v130, v130, v185, s68\n"
      "v_fma_f32 v131, v131, v185, s68\n"
      "v_exp_f32_e64 v224, v224 clamp\n"
      "v_exp_f32_e64 v225, v225 clamp\n"
      "s_add_u32 m0, s15, 0x18000\n"
      "v_add_u32 v4, 0x70000, v4\n"
      "buffer_load_dwordx4 v4, s[44:47], 0 offen offset:0 lds\n"
      "s_waitcnt lgkmcnt(2)\n"
      "v_mfma_f32_16x16x32_bf16 v[144:147], v[196:199], a[208:211], v[144:147]\n"
      "v_exp_f32_e64 v226, v226 clamp\n"
      "s_waitcnt vmcnt(16) lgkmcnt(0)\n"
      "s_barrier\n"
      "ds_read_b64_tr_b16 a[192:193], v193 offset:0\n"
      "ds_read_b64_tr_b16 a[194:195], v193 offset:1024\n"
      "buffer_load_dwordx4 v4, s[44:47], s13 offen offset:1024 lds\n"
      "v_exp_f32_e64 v227, v227 clamp\n"
      "v_max_f32 v128, s68, v128\n"
      "v_max_f32 v129, s68, v129\n"
      "v_max_f32 v130, s68, v130\n"
      "v_max_f32 v131, s68, v131\n"
      "v_mfma_f32_16x16x32_bf16 v[144:147], v[196:199], a[212:215], v[144:147]\n"
      "ds_read_b64_tr_b16 a[196:197], v193 offset:2048\n"
      "ds_read_b64_tr_b16 a[198:199], v193 offset:3072\n"
      "v_add_f32 v128, v128, v224\n"
      "v_add_f32 v129, v129, v225\n"
      "v_add_f32 v130, v130, v226\n"
      "v_add_f32 v131, v131, v227\n"
      "v_cvt_pk_bf16_f32 v88, v128, v129\n"
      "v_cvt_pk_bf16_f32 v89, v130, v131\n"
      "buffer_load_dwordx4 v4, s[44:47], s14 offen offset:2048 lds\n"
      "v_mul_f32 v224, v202, v132\n"
      "v_mfma_f32_16x16x32_bf16 v[144:147], v[196:199], a[216:219], v[144:147]\n"
      "ds_read_b64_tr_b16 a[200:201], v193 offset:4096\n"
      "ds_read_b64_tr_b16 a[202:203], v193 offset:5120\n"
      "v_mul_f32 v225, v202, v133\n"
      "v_mul_f32 v226, v202, v134\n"
      "v_mul_f32 v227, v202, v135\n"
      "v_fma_f32 v132, v132, v185, s68\n"
      "v_fma_f32 v133, v133, v185, s68\n"
      "v_fma_f32 v134, v134, v185, s68\n"
      "s_mov_b64 exec, 0xffff\n"
      "buffer_load_dwordx2 v[24:25], v201, s[56:59], s80 offen sc1\n"
      "s_mov_b64 exec, -1\n"
      "v_mfma_f32_16x16x32_bf16 v[144:147], v[196:199], a[220:223], v[144:147]\n"
      "ds_read_b64_tr_b16 a[204:205], v193 offset:6144\n"
      "ds_read_b64_tr_b16 a[206:207], v193 offset:7168\n"
      "v_fma_f32 v135, v135, v185, s68\n"
      "v_exp_f32_e64 v224, v224 clamp\n"
      "v_exp_f32_e64 v225, v225 clamp\n"
      "v_exp_f32_e64 v226, v226 clamp\n"
      "v_exp_f32_e64 v227, v227 clamp\n"
      "v_max_f32 v132, s68, v132\n"
      "s_waitcnt lgkmcnt(2)\n"
      "v_mfma_f32_16x16x32_bf16 v[144:147], v[196:199], a[192:195], v[144:147]\n"
      "buffer_load_dwordx4 v4, s[44:47], s2 offen offset:3072 lds\n"
      "v_max_f32 v133, s68, v133\n"
      "v_max_f32 v134, s68, v134\n"
      "v_max_f32 v135, s68, v135\n"
      "v_add_f32 v132, v132, v224\n"
      "v_add_f32 v133, v133, v225\n"
      "v_add_f32 v134, v134, v226\n"
      "v_mfma_f32_16x16x32_bf16 v[144:147], v[196:199], a[196:199], v[144:147]\n"
      "ds_read_b64_tr_b16 a[208:209], v192 offset:0\n"
      "ds_read_b64_tr_b16 a[210:211], v192 offset:1024\n"
      "ds_read_b64_tr_b16 a[212:213], v192 offset:2048\n"
      "ds_read_b64_tr_b16 a[214:215], v192 offset:3072\n"
      "v_add_f32 v135, v135, v227\n"
      "v_cvt_pk_bf16_f32 v90, v132, v133\n"
      "v_cvt_pk_bf16_f32 v91, v134, v135\n"
      "v_mul_f32 v224, v202, v136\n"
      "s_add_u32 m0, s15, 0x1c000\n"
      "s_nop 0\n"
      "buffer_load_dwordx4 v23, s[48:51], 0 offen offset:0 lds\n"
      "v_mul_f32 v225, v202, v137\n"
      "v_mul_f32 v226, v202, v138\n"
      "v_mul_f32 v227, v202, v139\n"
      "v_mfma_f32_16x16x32_bf16 v[144:147], v[196:199], a[200:203], v[144:147]\n"
      "ds_read_b64_tr_b16 a[216:217], v192 offset:4096\n"
      "ds_read_b64_tr_b16 a[218:219], v192 offset:5120\n"
      "v_fma_f32 v136, v136, v185, s68\n"
      "v_fma_f32 v137, v137, v185, s68\n"
      "v_fma_f32 v138, v138, v185, s68\n"
      "v_fma_f32 v139, v139, v185, s68\n"
      "v_exp_f32_e64 v224, v224 clamp\n"
      "v_exp_f32_e64 v225, v225 clamp\n"
      "s_waitcnt lgkmcnt(4)\n"
      "v_mfma_f32_16x16x32_bf16 v[144:147], v[196:199], a[204:207], v[144:147]\n"
      "buffer_load_dwordx4 v23, s[48:51], s16 offen offset:1024 lds\n"
      "v_exp_f32_e64 v226, v226 clamp\n"
      "v_exp_f32_e64 v227, v227 clamp\n"
      "v_max_f32 v136, s68, v136\n"
      "v_max_f32 v137, s68, v137\n"
      "v_max_f32 v138, s68, v138\n"
      "v_max_f32 v139, s68, v139\n"
      "v_mfma_f32_16x16x32_bf16 v[144:147], v[196:199], a[208:211], v[144:147]\n"
      "ds_read_b64_tr_b16 a[220:221], v192 offset:6144\n"
      "ds_read_b64_tr_b16 a[222:223], v192 offset:7168\n"
      "v_add_f32 v136, v136, v224\n"
      "v_add_f32 v137, v137, v225\n"
      "v_add_f32 v138, v138, v226\n"
      "v_add_f32 v139, v139, v227\n"
      "buffer_load_dwordx4 v23, s[48:51], s17 offen offset:2048 lds\n"
      "v_cvt_pk_bf16_f32 v92, v136, v137\n"
      "v_cvt_pk_bf16_f32 v93, v138, v139\n"
      "v_mul_f32 v224, v202, v140\n"
      "s_waitcnt lgkmcnt(2)\n"
      "v_mfma_f32_16x16x32_bf16 v[144:147], v[196:199], a[212:215], v[144:147]\n"
      "ds_read_b64_tr_b16 a[192:193], v15 offset:16384\n"
      "ds_read_b64_tr_b16 a[194:195], v15 offset:20480\n"
      "ds_read_b64_tr_b16 a[196:197], v18 offset:16384\n"
      "ds_read_b64_tr_b16 a[198:199], v18 offset:20480\n"
      "v_mul_f32 v225, v202, v141\n"
      "v_mul_f32 v226, v202, v142\n"
      "v_mul_f32 v227, v202, v143\n"
      "v_fma_f32 v140, v140, v185, s68\n"
      "v_fma_f32 v141, v141, v185, s68\n"
      "v_fma_f32 v142, v142, v185, s68\n"
      "buffer_load_dwordx4 v23, s[48:51], s18 offen offset:3072 lds\n"
      "v_mfma_f32_16x16x32_bf16 v[144:147], v[196:199], a[216:219], v[144:147]\n"
      "ds_read_b64_tr_b16 a[200:201], v15 offset:24576\n"
      "ds_read_b64_tr_b16 a[202:203], v15 offset:28672\n"
      "v_fma_f32 v143, v143, v185, s68\n"
      "v_exp_f32_e64 v224, v224 clamp\n"
      "v_exp_f32_e64 v225, v225 clamp\n"
      "v_exp_f32_e64 v226, v226 clamp\n"
      "v_exp_f32_e64 v227, v227 clamp\n"
      "v_max_f32 v140, s68, v140\n"
      "s_mov_b64 exec, 0xffff\n"
      "s_waitcnt vmcnt(5)\n"
      "v_cmp_eq_u64 vcc, s[60:61], v[24:25]\n"
      "s_nop 0\n"
      "s_cmp_eq_u64 vcc, exec\n"
      "s_cbranch_scc0 L_slow_x\n"
      "L_ok_x:\n"
      "s_mov_b64 exec, -1\n"
      "s_waitcnt lgkmcnt(0)\n"
      "v_mfma_f32_16x16x32_bf16 v[144:147], v[196:199], a[220:223], v[144:147]\n"
      "s_add_u32 s78, s77, 0x600000\n"
      "buffer_load_dwordx4 a[0:3], v174, s[56:59], s78 offen offset:0 sc1\n"
      "v_max_f32 v141, s68, v141\n"
      "v_max_f32 v142, s68, v142\n"
      "v_max_f32 v143, s68, v143\n"
      "v_add_f32 v140, v140, v224\n"
      "v_add_f32 v141, v141, v225\n"
      "v_add_f32 v142, v142, v226\n"
      "v_add_f32 v143, v143, v227\n"
      "v_cvt_pk_bf16_f32 v94, v140, v141\n"
      "v_cvt_pk_bf16_f32 v95, v142, v143\n"
      "buffer_load_dwordx4 a[4:7], v174, s[56:59], s78 offen offset:1024 sc1\n"
      "buffer_load_dwordx4 a[8:11], v174, s[56:59], s78 offen offset:2048 sc1\n"
      "buffer_load_dwordx4 a[12:15], v174, s[56:59], s78 offen offset:3072 sc1\n"
      "s_add_u32 s78, s77, 0x601000\n"
      "buffer_load_dwordx4 a[16:19], v174, s[56:59], s78 offen offset:0 sc1\n"
      "buffer_load_dwordx4 a[20:23], v174, s[56:59], s78 offen offset:1024 sc1\n"
      "buffer_load_dwordx4 a[24:27], v174, s[56:59], s78 offen offset:2048 sc1\n"
      "buffer_load_dwordx4 a[28:31], v174, s[56:59], s78 offen offset:3072 sc1\n"
      "s_add_u32 s78, s77, 0x602000\n"
      "buffer_load_dwordx4 a[32:35], v174, s[56:59], s78 offen offset:0 sc1\n"
      "buffer_load_dwordx4 a[36:39], v174, s[56:59], s78 offen offset:1024 sc1\n"
      "s_add_u32 s78, s94, 0x140000\n"
      "buffer_load_dwordx4 v[64:67], v1, s[56:59], s78 offen offset:0 sc1\n"
      "buffer_load_dwordx4 v[68:71], v1, s[56:59], s78 offen offset:1024 sc1\n"
      "buffer_load_dwordx4 v[72:75], v1, s[56:59], s78 offen offset:2048 sc1\n"
      "s_waitcnt vmcnt(12)\n"
      "v_mfma_f32_32x32x16_bf16 v[96:111], a[192:195], a[0:3], 0\n"
      "ds_read_b64_tr_b16 a[204:205], v18 offset:24576\n"
      "ds_read_b64_tr_b16 a[206:207], v18 offset:28672\n"
      "ds_read_b64_tr_b16 a[208:209], v15 offset:17408\n"
      "ds_read_b64_tr_b16 a[210:211], v15 offset:21504\n"
      "v_mfma_f32_32x32x16_bf16 v[112:127], a[196:199], a[0:3], 0\n"
      "buffer_load_dwordx4 v[76:79], v1, s[56:59], s78 offen offset:3072 sc1\n"
      "s_waitcnt vmcnt(12)\n"
      "v_mfma_f32_32x32x16_bf16 v[96:111], a[200:203], a[4:7], v[96:111]\n"
      "ds_read_b64_tr_b16 a[212:213], v18 offset:17408\n"
      "ds_read_b64_tr_b16 a[214:215], v18 offset:21504\n"
      "ds_read_b64_tr_b16 a[216:217], v15 offset:25600\n"
      "ds_read_b64_tr_b16 a[218:219], v15 offset:29696\n"
      "s_waitcnt lgkmcnt(6)\n"
      "v_mfma_f32_32x32x16_bf16 v[112:127], a[204:207], a[4:7], v[112:127]\n"
      "s_add_u32 s78, s77, 0x602000\n"
      "buffer_load_dwordx4 a[40:43], v174, s[56:59], s78 offen offset:2048 sc1\n"
      "ds_write_b32 v195, v144\n"
      "s_waitcnt vmcnt(12) lgkmcnt(5)\n"
      "v_mfma_f32_32x32x16_bf16 v[96:111], a[208:211], a[8:11], v[96:111]\n"
      "ds_read_b64_tr_b16 a[220:221], v18 offset:25600\n"
      "ds_read_b64_tr_b16 a[222:223], v18 offset:29696\n"
      "ds_read_b64_tr_b16 a[192:193], v15 offset:18432\n"
      "ds_read_b64_tr_b16 a[194:195], v15 offset:22528\n"
      "s_waitcnt lgkmcnt(7)\n"
      "v_mfma_f32_32x32x16_bf16 v[112:127], a[212:215], a[8:11], v[112:127]\n"
      "buffer_load_dwordx4 a[44:47], v174, s[56:59], s78 offen offset:3072 sc1\n"
      "s_waitcnt vmcnt(12) lgkmcnt(5)\n"
      "v_mfma_f32_32x32x16_bf16 v[96:111], a[216:219], a[12:15], v[96:111]\n"
      "ds_read_b64_tr_b16 a[196:197], v18 offset:18432\n"
      "ds_read_b64_tr_b16 a[198:199], v18 offset:22528\n"
      "ds_read_b64_tr_b16 a[200:201], v15 offset:26624\n"
      "ds_read_b64_tr_b16 a[202:203], v15 offset:30720\n"
      "s_waitcnt lgkmcnt(6)\n"
      "v_mfma_f32_32x32x16_bf16 v[112:127], a[220:223], a[12:15], v[112:127]\n"
      "s_add_u32 s78, s77, 0x603000\n"
      "buffer_load_dwordx4 a[48:51], v174, s[56:59], s78 offen offset:0 sc1\n"
      "s_waitcnt vmcnt(12) lgkmcnt(4)\n"
      "v_mfma_f32_32x32x16_bf16 v[96:111], a[192:195], a[16:19], v[96:111]\n"
      "ds_read_b64_tr_b16 a[204:205], v18 offset:26624\n"
      "ds_read_b64_tr_b16 a[206:207], v18 offset:30720\n"
      "ds_read_b64_tr_b16 a[208:209], v15 offset:19456\n"
      "ds_read_b64_tr_b16 a[210:211], v15 offset:23552\n"
      "s_waitcnt lgkmcnt(6)\n"
      "v_mfma_f32_32x32x16_bf16 v[112:127], a[196:199], a[16:19], v[112:127]\n"
      "buffer_load_dwordx4 a[52:55], v174, s[56:59], s78 offen offset:1024 sc1\n"
      "s_waitcnt vmcnt(12) lgkmcnt(4)\n"
      "v_mfma_f32_32x32x16_bf16 v[96:111], a[200:203], a[20:23], v[96:111]\n"
      "ds_read_b64_tr_b16 a[212:213], v18 offset:19456\n"
      "ds_read_b64_tr_b16 a[214:215], v18 offset:23552\n"
      "ds_read_b64_tr_b16 a[216:217], v15 offset:27648\n"
      "ds_read_b64_tr_b16 a[218:219], v15 offset:31744\n"
      "s_waitcnt lgkmcnt(6)\n"
      "v_mfma_f32_32x32x16_bf16 v[112:127], a[204:207], a[20:23], v[112:127]\n"
      "buffer_load_dwordx4 a[56:59], v174, s[56:59], s78 offen offset:2048 sc1\n"
      "s_waitcnt vmcnt(12) lgkmcnt(4)\n"
      "v_mfma_f32_32x32x16_bf16 v[96:111], a[208:211], a[24:27], v[96:111]\n"
      "ds_read_b64_tr_b16 a[220:221], v18 offset:27648\n"
      "ds_read_b64_tr_b16 a[222:223], v18 offset:31744\n"
      "ds_read_b64_tr_b16 a[192:193], v15 offset:49152\n"
      "ds_read_b64_tr_b16 a[194:195], v15 offset:53248\n"
      "s_waitcnt lgkmcnt(6)\n"
      "v_mfma_f32_32x32x16_bf16 v[112:127], a[212:215], a[24:27], v[112:127]\n"
      "buffer_load_dwordx4 a[60:63], v174, s[56:59], s78 offen offset:3072 sc1\n"
      "s_waitcnt vmcnt(12) lgkmcnt(4)\n"
      "v_mfma_f32_32x32x16_bf16 v[96:111], a[216:219], a[28:31], v[96:111]\n"
      "ds_read_b64_tr_b16 a[196:197], v18 offset:49152\n"
      "ds_read_b64_tr_b16 a[198:199], v18 offset:53248\n"
      "ds_read_b64_tr_b16 a[200:201], v15 offset:57344\n"
      "ds_read_b64_tr_b16 a[202:203], v15 offset:61440\n"
      "s_waitcnt lgkmcnt(6)\n"
      "v_mfma_f32_32x32x16_bf16 v[112:127], a[220:223], a[28:31], v[112:127]\n"
      "s_add_u32 s78, s77, 0x604000\n"
      "buffer_load_dwordx4 a[64:67], v174, s[56:59], s78 offen offset:0 sc1\n"
      "s_waitcnt vmcnt(12) lgkmcnt(4)\n"
      "v_mfma_f32_32x32x16_bf16 v[96:111], a[192:195], a[32:35], v[96:111]\n"
      "ds_read_b64_tr_b16 a[204:205], v18 offset:57344\n"
      "ds_read_b64_tr_b16 a[206:207], v18 offset:61440\n"
      "ds_read_b64_tr_b16 a[208:209], v15 offset:50176\n"
      "ds_read_b64_tr_b16 a[210:211], v15 offset:54272\n"
      "s_waitcnt lgkmcnt(6)\n"
      "v_mfma_f32_32x32x16_bf16 v[112:127], a[196:199], a[32:35], v[112:127]\n"
      "buffer_load_dwordx4 a[68:71], v174, s[56:59], s78 offen offset:1024 sc1\n"
      "s_waitcnt vmcnt(12) lgkmcnt(4)\n"
      "v_mfma_f32_32x32x16_bf16 v[96:111], a[200:203], a[36:39], v[96:111]\n"
      "ds_read_b64_tr_b16 a[212:213], v18 offset:50176\n"
      "ds_read_b64_tr_b16 a[214:215], v18 offset:54272\n"
      "ds_read_b64_tr_b16 a[216:217], v15 offset:58368\n"
      "ds_read_b64_tr_b16 a[218:219], v15 offset:62464\n"
      "s_waitcnt lgkmcnt(6)\n"
      "v_mfma_f32_32x32x16_bf16 v[112:127], a[204:207], a[36:39], v[112:127]\n"
      "buffer_load_dwordx4 a[72:75], v174, s[56:59], s78 offen offset:2048 sc1\n"
      "s_waitcnt lgkmcnt(4)\n"
      "buffer_load_dwordx4 a[76:79], v174, s[56:59], s78 offen offset:3072 sc1\n"
      "s_add_u32 s78, s77, 0x605000\n"
      "buffer_load_dwordx4 a[80:83], v174, s[56:59], s78 offen offset:0 sc1\n"
      "buffer_load_dwordx4 a[84:87], v174, s[56:59], s78 offen offset:1024 sc1\n"
      "buffer_load_dwordx4 a[88:91], v174, s[56:59], s78 offen offset:2048 sc1\n"
      "s_waitcnt vmcnt(12)\n"
      "v_mfma_f32_32x32x16_bf16 v[96:111], a[208:211], a[40:43], v[96:111]\n"
      "ds_read_b64_tr_b16 a[220:221], v18 offset:58368\n"
      "ds_read_b64_tr_b16 a[222:223], v18 offset:62464\n"
      "ds_read_b64_tr_b16 a[192:193], v15 offset:51200\n"
      "ds_read_b64_tr_b16 a[194:195], v15 offset:55296\n"
      "v_add_f32 v64, v64, v68\n"
      "s_waitcnt lgkmcnt(4)\n"
      "v_mfma_f32_32x32x16_bf16 v[112:127], a[212:215], a[40:43], v[112:127]\n"
      "buffer_load_dwordx4 a[92:95], v174, s[56:59], s78 offen offset:3072 sc1\n"
      "s_waitcnt vmcnt(12)\n"
      "v_mfma_f32_32x32x16_bf16 v[96:111], a[216:219], a[44:47], v[96:111]\n"
      "ds_read_b64_tr_b16 a[196:197], v18 offset:51200\n"
      "ds_read_b64_tr_b16 a[198:199], v18 offset:55296\n"
      "ds_read_b64_tr_b16 a[200:201], v15 offset:59392\n"
      "ds_read_b64_tr_b16 a[202:203], v15 offset:63488\n"
      "v_add_f32 v72, v72, v76\n"
      "s_waitcnt lgkmcnt(6)\n"
      "v_mfma_f32_32x32x16_bf16 v[112:127], a[220:223], a[44:47], v[112:127]\n"
      "s_add_u32 s78, s77, 0x606000\n"
      "buffer_load_dwordx4 a[96:99], v174, s[56:59], s78 offen offset:0 sc1\n"
      "v_add_f32 v65, v65, v69\n"
      "s_waitcnt vmcnt(12) lgkmcnt(4)\n"
      "v_mfma_f32_32x32x16_bf16 v[96:111], a[192:195], a[48:51], v[96:111]\n"
      "ds_read_b64_tr_b16 a[204:205], v18 offset:59392\n"
      "ds_read_b64_tr_b16 a[206:207], v18 offset:63488\n"
      "ds_read_b64_tr_b16 a[208:209], v15 offset:52224\n"
      "ds_read_b64_tr_b16 a[210:211], v15 offset:56320\n"
      "v_add_f32 v73, v73, v77\n"
      "v_add_f32 v66, v66, v70\n"
      "s_waitcnt lgkmcnt(6)\n"
      "v_mfma_f32_32x32x16_bf16 v[112:127], a[196:199], a[48:51], v[112:127]\n"
      "buffer_load_dwordx4 a[100:103], v174, s[56:59], s78 offen offset:1024 sc1\n"
      "v_add_f32 v74, v74, v78\n"
      "v_add_f32 v67, v67, v71\n"
      "s_waitcnt vmcnt(12) lgkmcnt(4)\n"
      "v_mfma_f32_32x32x16_bf16 v[96:111], a[200:203], a[52:55], v[96:111]\n"
      "ds_read_b64_tr_b16 a[212:213], v18 offset:52224\n"
      "ds_read_b64_tr_b16 a[214:215], v18 offset:56320\n"
      "ds_read_b64_tr_b16 a[216:217], v15 offset:60416\n"
      "ds_read_b64_tr_b16 a[218:219], v15 offset:64512\n"
      "v_add_f32 v75, v75, v79\n"
      "v_add_f32 v64, v64, v72\n"
      "s_waitcnt lgkmcnt(6)\n"
      "v_mfma_f32_32x32x16_bf16 v[112:127], a[204:207], a[52:55], v[112:127]\n"
      "buffer_load_dwordx4 a[104:107], v174, s[56:59], s78 offen offset:2048 sc1\n"
      "v_add_f32 v65, v65, v73\n"
      "s_waitcnt vmcnt(12) lgkmcnt(4)\n"
      "v_mfma_f32_32x32x16_bf16 v[96:111], a[208:211], a[56:59], v[96:111]\n"
      "ds_read_b64_tr_b16 a[220:221], v18 offset:60416\n"
      "ds_read_b64_tr_b16 a[222:223], v18 offset:64512\n"
      "v_add_f32 v66, v66, v74\n"
      "v_add_f32 v67, v67, v75\n"
      "s_nop 0\n"
      "v_permlane32_swap_b32 v64, v66\n"
      "s_waitcnt lgkmcnt(4)\n"
      "v_mfma_f32_32x32x16_bf16 v[112:127], a[212:215], a[56:59], v[112:127]\n"
      "s_waitcnt lgkmcnt(0)\n"
      "s_barrier\n"
      "ds_read_b64_tr_b16 a[192:193], v17 offset:0\n"
      "ds_read_b64_tr_b16 a[194:195], v17 offset:4096\n"
      "buffer_load_dwordx4 a[108:111], v174, s[56:59], s78 offen offset:3072 sc1\n"
      "v_permlane32_swap_b32 v65, v67\n"
      "s_waitcnt vmcnt(12)\n"
      "v_mfma_f32_32x32x16_bf16 v[96:111], a[216:219], a[60:63], v[96:111]\n"
      "ds_read_b64_tr_b16 a[196:197], v20 offset:0\n"
      "ds_read_b64_tr_b16 a[198:199], v20 offset:4096\n"
      "ds_read_b64_tr_b16 a[200:201], v17 offset:8192\n"
      "ds_read_b64_tr_b16 a[202:203], v17 offset:12288\n"
      "v_add_f32 v64, v64, v66\n"
      "v_add_f32 v65, v65, v67\n"
      "v_mfma_f32_32x32x16_bf16 v[112:127], a[220:223], a[60:63], v[112:127]\n"
      "s_add_u32 s78, s77, 0x607000\n"
      "buffer_load_dwordx4 a[112:115], v174, s[56:59], s78 offen offset:0 sc1\n"
      "v_permlane16_swap_b32 v64, v65\n"
      "v_add_f32 v64, v64, v65\n"
      "v_mul_f32 v64, 0x3b000000, v64\n"
      "v_mov_b32 v65, v64\n"
      "s_waitcnt vmcnt(12) lgkmcnt(4)\n"
      "v_mfma_f32_32x32x16_bf16 v[96:111], a[192:195], a[64:67], v[96:111]\n"
      "ds_read_b64_tr_b16 a[204:205], v20 offset:8192\n"
      "ds_read_b64_tr_b16 a[206:207], v20 offset:12288\n"
      "ds_read_b64_tr_b16 a[208:209], v17 offset:1024\n"
      "ds_read_b64_tr_b16 a[210:211], v17 offset:5120\n"
      "ds_read_b32 v206, v194 offset:0\n"
      "s_waitcnt lgkmcnt(7)\n"
      "v_mfma_f32_32x32x16_bf16 v[112:127], a[196:199], a[64:67], v[112:127]\n"
      "buffer_load_dwordx4 a[116:119], v174, s[56:59], s78 offen offset:1024 sc1\n"
      "s_waitcnt vmcnt(12) lgkmcnt(5)\n"
      "v_mfma_f32_32x32x16_bf16 v[96:111], a[200:203], a[68:71], v[96:111]\n"
      "ds_read_b64_tr_b16 a[212:213], v20 offset:1024\n"
      "ds_read_b64_tr_b16 a[214:215], v20 offset:5120\n"
      "ds_read_b64_tr_b16 a[216:217], v17 offset:9216\n"
      "ds_read_b64_tr_b16 a[218:219], v17 offset:13312\n"
      "ds_read_b32 v207, v194 offset:256\n"
      "s_waitcnt lgkmcnt(8)\n"
      "v_mfma_f32_32x32x16_bf16 v[112:127], a[204:207], a[68:71], v[112:127]\n"
      "buffer_load_dwordx4 a[120:123], v174, s[56:59], s78 offen offset:2048 sc1\n"
      "s_waitcnt vmcnt(12) lgkmcnt(6)\n"
      "v_mfma_f32_32x32x16_bf16 v[96:111], a[208:211], a[72:75], v[96:111]\n"
      "ds_read_b64_tr_b16 a[220:221], v20 offset:9216\n"
      "ds_read_b64_tr_b16 a[222:223], v20 offset:13312\n"
      "ds_read_b64_tr_b16 a[192:193], v17 offset:2048\n"
      "ds_read_b64_tr_b16 a[194:195], v17 offset:6144\n"
      "ds_read_b32 v210, v194 offset:128\n"
      "s_waitcnt lgkmcnt(8)\n"
      "v_mfma_f32_32x32x16_bf16 v[112:127], a[212:215], a[72:75], v[112:127]\n"
      "buffer_load_dwordx4 a[124:127], v174, s[56:59], s78 offen offset:3072 sc1\n"
      "ds_read_b32 v211, v194 offset:384\n"
      "s_waitcnt lgkmcnt(6)\n"
      "v_add_f32 v206, v206, v207\n"
      "s_waitcnt vmcnt(12)\n"
      "v_mfma_f32_32x32x16_bf16 v[96:111], a[216:219], a[76:79], v[96:111]\n"
      "ds_read_b64_tr_b16 a[196:197], v20 offset:2048\n"
      "ds_read_b64_tr_b16 a[198:199], v20 offset:6144\n"
      "ds_read_b64_tr_b16 a[200:201], v17 offset:10240\n"
      "ds_read_b64_tr_b16 a[202:203], v17 offset:14336\n"
      "v_cvt_pk_bf16_f32 v205, v206, 0\n"
      "s_waitcnt lgkmcnt(8)\n"
      "v_mfma_f32_32x32x16_bf16 v[112:127], a[220:223], a[76:79], v[112:127]\n"
      "s_add_u32 m0, s15, 0x4000\n"
      "v_add_u32 v23, 0x20000, v23\n"
      "buffer_load_dwordx4 v23, s[48:51], 0 offen offset:0 lds\n"
      "v_lshlrev_b32 v207, 16, v205\n"
      "s_waitcnt vmcnt(12) lgkmcnt(6)\n"
      "v_mfma_f32_32x32x16_bf16 v[96:111], a[192:195], a[80:83], v[96:111]\n"
      "ds_read_b64_tr_b16 a[204:205], v20 offset:10240\n"
      "ds_read_b64_tr_b16 a[206:207], v20 offset:14336\n"
      "ds_read_b64_tr_b16 a[208:209], v17 offset:3072\n"
      "ds_read_b64_tr_b16 a[210:211], v17 offset:7168\n"
      "v_sub_f32 v207, v206, v207\n"
      "s_waitcnt lgkmcnt(6)\n"
      "v_mfma_f32_32x32x16_bf16 v[112:127], a[196:199], a[80:83], v[112:127]\n"
      "buffer_load_dwordx4 v23, s[48:51], s16 offen offset:1024 lds\n"
      "v_cvt_pk_bf16_f32 v204, v206, v207\n"
      "s_waitcnt vmcnt(12) lgkmcnt(4)\n"
      "v_mfma_f32_32x32x16_bf16 v[96:111], a[200:203], a[84:87], v[96:111]\n"
      "ds_read_b64_tr_b16 a[212:213], v20 offset:3072\n"
      "ds_read_b64_tr_b16 a[214:215], v20 offset:7168\n"
      "ds_read_b64_tr_b16 a[216:217], v17 offset:11264\n"
      "ds_read_b64_tr_b16 a[218:219], v17 offset:15360\n"
      "v_mov_b32 v206, 0\n"
      "v_mov_b32 v207, 0\n"
      "s_waitcnt lgkmcnt(6)\n"
      "v_mfma_f32_32x32x16_bf16 v[112:127], a[204:207], a[84:87], v[112:127]\n"
      "buffer_load_dwordx4 v23, s[48:51], s17 offen offset:2048 lds\n"
      "v_add_f32 v210, v210, v211\n"
      "s_waitcnt vmcnt(12) lgkmcnt(4)\n"
      "v_mfma_f32_32x32x16_bf16 v[96:111], a[208:211], a[88:91], v[96:111]\n"
      "ds_read_b64_tr_b16 a[220:221], v20 offset:11264\n"
      "ds_read_b64_tr_b16 a[222:223], v20 offset:15360\n"
      "ds_read_b64_tr_b16 a[192:193], v16 offset:0\n"
      "ds_read_b64_tr_b16 a[194:195], v16 offset:4096\n"
      "v_cvt_pk_bf16_f32 v209, v210, 0\n"
      "s_waitcnt lgkmcnt(6)\n"
      "v_mfma_f32_32x32x16_bf16 v[112:127], a[212:215], a[88:91], v[112:127]\n"
      "buffer_load_dwordx4 v23, s[48:51], s18 offen offset:3072 lds\n"
      "v_lshlrev_b32 v211, 16, v209\n"
      "s_waitcnt vmcnt(12) lgkmcnt(4)\n"
      "v_mfma_f32_32x32x16_bf16 v[96:111], a[216:219], a[92:95], v[96:111]\n"
      "ds_read_b64_tr_b16 a[196:197], v19 offset:0\n"
      "ds_read_b64_tr_b16 a[198:199], v19 offset:4096\n"
      "ds_read_b64_tr_b16 a[200:201], v16 offset:8192\n"
      "ds_read_b64_tr_b16 a[202:203], v16 offset:12288\n"
      "v_sub_f32 v211, v210, v211\n"
      "s_waitcnt lgkmcnt(6)\n"
      "v_mfma_f32_32x32x16_bf16 v[112:127], a[220:223], a[92:95], v[112:127]\n"
      "s_add_u32 m0, s15, 0xc000\n"
      "v_add_u32 v23, 0x20000, v23\n"
      "buffer_load_dwordx4 v23, s[48:51], 0 offen offset:0 lds\n"
      "v_cvt_pk_bf16_f32 v208, v210, v211\n"
      "v_mov_b32 v210, 0\n"
      "s_waitcnt vmcnt(12) lgkmcnt(4)\n"
      "v_mfma_f32_32x32x16_bf16 v[96:111], a[192:195], a[96:99], v[96:111]\n"
      "ds_read_b64_tr_b16 a[204:205], v19 offset:8192\n"
      "ds_read_b64_tr_b16 a[206:207], v19 offset:12288\n"
      "ds_read_b64_tr_b16 a[208:209], v16 offset:1024\n"
      "ds_read_b64_tr_b16 a[210:211], v16 offset:5120\n"
      "v_mov_b32 v211, 0\n"
      "s_waitcnt lgkmcnt(6)\n"
      "v_mfma_f32_32x32x16_bf16 v[112:127], a[196:199], a[96:99], v[112:127]\n"
      "buffer_load_dwordx4 v23, s[48:51], s16 offen offset:1024 lds\n"
      "v_permlane32_swap_b32 v64, v65\n"
      "s_waitcnt vmcnt(12) lgkmcnt(4)\n"
      "v_mfma_f32_32x32x16_bf16 v[96:111], a[200:203], a[100:103], v[96:111]\n"
      "ds_read_b64_tr_b16 a[212:213], v19 offset:1024\n"
      "ds_read_b64_tr_b16 a[214:215], v19 offset:5120\n"
      "ds_read_b64_tr_b16 a[216:217], v16 offset:9216\n"
      "ds_read_b64_tr_b16 a[218:219], v16 offset:13312\n"
      "v_fma_f32 v65, -v64, v64, v65\n"
      "s_waitcnt lgkmcnt(6)\n"
      "v_mfma_f32_32x32x16_bf16 v[112:127], a[204:207], a[100:103], v[112:127]\n"
      "buffer_load_dwordx4 v23, s[48:51], s17 offen offset:2048 lds\n"
      "v_max_f32 v65, 0, v65\n"
      "s_waitcnt vmcnt(12) lgkmcnt(4)\n"
      "v_mfma_f32_32x32x16_bf16 v[96:111], a[208:211], a[104:107], v[96:111]\n"
      "ds_read_b64_tr_b16 a[220:221], v19 offset:9216\n"
      "ds_read_b64_tr_b16 a[222:223], v19 offset:13312\n"
      "ds_read_b64_tr_b16 a[192:193], v16 offset:2048\n"
      "ds_read_b64_tr_b16 a[194:195], v16 offset:6144\n"
      "v_add_f32 v65, 0x3727c5ac, v65\n"
      "v_rsq_f32 v66, v65\n"
      "s_waitcnt lgkmcnt(6)\n"
      "v_mfma_f32_32x32x16_bf16 v[112:127], a[212:215], a[104:107], v[112:127]\n"
      "buffer_load_dwordx4 v23, s[48:51], s18 offen offset:3072 lds\n"
      "v_cvt_f32_u32 v67, v172\n"
      "s_waitcnt vmcnt(12) lgkmcnt(4)\n"
      "v_mfma_f32_32x32x16_bf16 v[96:111], a[216:219], a[108:111], v[96:111]\n"
      "ds_read_b64_tr_b16 a[196:197], v19 offset:2048\n"
      "ds_read_b64_tr_b16 a[198:199], v19 offset:6144\n"
      "ds_read_b64_tr_b16 a[200:201], v16 offset:10240\n"
      "ds_read_b64_tr_b16 a[202:203], v16 offset:14336\n"
      "v_fma_f32 v64, v67, v64, -v64\n"
      "s_waitcnt lgkmcnt(6)\n"
      "v_mfma_f32_32x32x16_bf16 v[112:127], a[220:223], a[108:111], v[112:127]\n"
      "ds_read_b64_tr_b16 a[204:205], v19 offset:10240\n"
      "ds_read_b64_tr_b16 a[206:207], v19 offset:14336\n"
      "v_cvt_pk_bf16_f32 v220, v64, v64\n"
      "ds_read_b64_tr_b16 a[208:209], v16 offset:3072\n"
      "ds_read_b64_tr_b16 a[210:211], v16 offset:7168\n"
      "s_waitcnt vmcnt(11) lgkmcnt(8)\n"
      "v_mfma_f32_32x32x16_bf16 v[96:111], a[192:195], a[112:115], v[96:111]\n"
      "v_lshlrev_b32 v67, 16, v220\n"
      "ds_read_b64_tr_b16 a[212:213], v19 offset:3072\n"
      "ds_read_b64_tr_b16 a[214:215], v19 offset:7168\n"
      "v_sub_f32 v67, v64, v67\n"
      "v_cvt_pk_bf16_f32 v221, v67, 0\n"
      "s_waitcnt lgkmcnt(8)\n"
      "v_mfma_f32_32x32x16_bf16 v[112:127], a[196:199], a[112:115], v[112:127]\n"
      "ds_read_b64_tr_b16 a[216:217], v16 offset:11264\n"
      "ds_read_b64_tr_b16 a[218:219], v16 offset:15360\n"
      "v_mov_b32 v222, 0\n"
      "v_mov_b32 v223, 0\n"
      "v_rcp_f32 v30, v66\n"
      "s_waitcnt vmcnt(10) lgkmcnt(8)\n"
      "v_mfma_f32_32x32x16_bf16 v[96:111], a[200:203], a[116:119], v[96:111]\n"
      "ds_read_b64_tr_b16 a[220:221], v19 offset:11264\n"
      "ds_read_b64_tr_b16 a[222:223], v19 offset:15360\n"
      "v_mul_f32 v67, v66, v66\n"
      "v_mul_f32 v28, 0xc0135761, v66\n"
      "v_mul_f32 v67, v67, v66\n"
      "s_waitcnt lgkmcnt(6)\n"
      "v_mfma_f32_32x32x16_bf16 v[112:127], a[204:207], a[116:119], v[112:127]\n"
      "v_mul_f32 v29, 0xbdd2d3e8, v67\n"
      "s_waitcnt vmcnt(9)\n"
      "v_mfma_f32_32x32x16_bf16 v[96:111], a[208:211], a[120:123], v[96:111]\n"
      "s_waitcnt lgkmcnt(2)\n"
      "v_mfma_f32_32x32x16_bf16 v[112:127], a[212:215], a[120:123], v[112:127]\n"
      "s_waitcnt lgkmcnt(0)\n"
      "s_barrier\n"
      "ds_read_b64_tr_b16 a[192:193], v192 offset:16384\n"
      "ds_read_b64_tr_b16 a[194:195], v192 offset:17408\n"
      "s_add_u32 m0, s15, 0x20000\n"
      "v_add_u32 v23, 0x20000, v23\n"
      "buffer_load_dwordx4 v23, s[48:51], 0 offen offset:0 lds\n"
      "s_waitcnt vmcnt(9)\n"
      "v_mfma_f32_32x32x16_bf16 v[96:111], a[216:219], a[124:127], v[96:111]\n"
      "ds_read_b64_tr_b16 a[196:197], v192 offset:18432\n"
      "ds_read_b64_tr_b16 a[198:199], v192 offset:19456\n"
      "ds_read_b64_tr_b16 a[200:201], v192 offset:20480\n"
      "ds_read_b64_tr_b16 a[202:203], v192 offset:21504\n"
      "v_mfma_f32_32x32x16_bf16 v[112:127], a[220:223], a[124:127], v[112:127]\n"
      "buffer_load_dwordx4 v23, s[48:51], s16 offen offset:1024 lds\n"
      "ds_read_b64_tr_b16 a[204:205], v192 offset:22528\n"
      "ds_read_b64_tr_b16 a[206:207], v192 offset:23552\n"
      "ds_read_b64_tr_b16 a[208:209], v191 offset:0\n"
      "ds_read_b64_tr_b16 a[210:211], v191 offset:1024\n"
      "ds_read_b64_tr_b16 a[212:213], v191 offset:2048\n"
      "ds_read_b64_tr_b16 a[214:215], v191 offset:3072\n"
      "v_mfma_f32_32x32x16_bf16 v[96:111], v[204:207], v[220:223], v[96:111]\n"
      "v_mfma_f32_32x32x16_bf16 v[112:127], v[208:211], v[220:223], v[112:127]\n"
      "buffer_load_dwordx4 v23, s[48:51], s17 offen offset:2048 lds\n"
      "s_add_u32 s23, s76, 0xa00000\n"
      "s_nop 8\n"
      "v_pk_mul_f32 v[224:225], v[96:97], v[96:97]\n"
      "v_pk_mul_f32 v[226:227], v[98:99], v[98:99]\n"
      "v_pk_mul_f32 v[228:229], v[100:101], v[100:101]\n"
      "v_pk_mul_f32 v[230:231], v[102:103], v[102:103]\n"
      "v_pk_fma_f32 v[224:225], v[224:225], v[28:29], v[28:29] op_sel:[0,1,0] op_sel_hi:[1,1,0]\n"
      "v_pk_fma_f32 v[226:227], v[226:227], v[28:29], v[28:29] op_sel:[0,1,0] op_sel_hi:[1,1,0]\n"
      "v_pk_fma_f32 v[228:229], v[228:229], v[28:29], v[28:29] op_sel:[0,1,0] op_sel_hi:[1,1,0]\n"
      "v_pk_fma_f32 v[230:231], v[230:231], v[28:29], v[28:29] op_sel:[0,1,0] op_sel_hi:[1,1,0]\n"
      "v_pk_mul_f32 v[224:225], v[224:225], v[96:97]\n"
      "v_pk_mul_f32 v[226:227], v[226:227], v[98:99]\n"
      "v_pk_mul_f32 v[228:229], v[228:229], v[100:101]\n"
      "buffer_load_dwordx4 v23, s[48:51], s18 offen offset:3072 lds\n"
      "v_pk_mul_f32 v[230:231], v[230:231], v[102:103]\n"
      "v_exp_f32 v224, v224\n"
      "v_exp_f32 v225, v225\n"
      "v_exp_f32 v226, v226\n"
      "v_exp_f32 v227, v227\n"
      "v_exp_f32 v228, v228\n"
      "v_exp_f32 v229, v229\n"
      "s_add_u32 m0, s15, 0x10000\n"
      "v_add_u32 v23, 0x20000, v23\n"
      "buffer_load_dwordx4 v23, s[48:51], 0 offen offset:0 lds\n"
      "v_exp_f32 v230, v230\n"
      "v_exp_f32 v231, v231\n"
      "v_pk_fma_f32 v[224:225], v[224:225], v[30:31], v[30:31] op_sel_hi:[1,0,0]\n"
      "v_pk_fma_f32 v[226:227], v[226:227], v[30:31], v[30:31] op_sel_hi:[1,0,0]\n"
      "v_pk_fma_f32 v[228:229], v[228:229], v[30:31], v[30:31] op_sel_hi:[1,0,0]\n"
      "v_pk_fma_f32 v[230:231], v[230:231], v[30:31], v[30:31] op_sel_hi:[1,0,0]\n"
      "v_rcp_f32 v224, v224\n"
      "v_rcp_f32 v225, v225\n"
      "v_rcp_f32 v226, v226\n"
      "buffer_load_dwordx4 v23, s[48:51], s16 offen offset:1024 lds\n"
      "v_rcp_f32 v227, v227\n"
      "v_rcp_f32 v228, v228\n"
      "v_rcp_f32 v229, v229\n"
      "v_rcp_f32 v230, v230\n"
      "v_rcp_f32 v231, v231\n"
      "v_pk_mul_f32 v[224:225], v[224:225], v[96:97]\n"
      "v_pk_mul_f32 v[226:227], v[226:227], v[98:99]\n"
      "v_pk_mul_f32 v[228:229], v[228:229], v[100:101]\n"
      "buffer_load_dwordx4 v23, s[48:51], s17 offen offset:2048 lds\n"
      "v_pk_mul_f32 v[230:231], v[230:231], v[102:103]\n"
      "v_cvt_pk_bf16_f32 v232, v224, v225\n"
      "v_cvt_pk_bf16_f32 v233, v226, v227\n"
      "v_cvt_pk_bf16_f32 v234, v228, v229\n"
      "v_cvt_pk_bf16_f32 v235, v230, v231\n"
      "buffer_store_dwordx4 v[232:235], v174, s[56:59], s23 offen offset:0 sc1\n"
      "v_pk_mul_f32 v[224:225], v[104:105], v[104:105]\n"
      "v_pk_mul_f32 v[226:227], v[106:107], v[106:107]\n"
      "v_pk_mul_f32 v[228:229], v[108:109], v[108:109]\n"
      "v_pk_mul_f32 v[230:231], v[110:111], v[110:111]\n"
      "v_pk_fma_f32 v[224:225], v[224:225], v[28:29], v[28:29] op_sel:[0,1,0] op_sel_hi:[1,1,0]\n"
      "v_pk_fma_f32 v[226:227], v[226:227], v[28:29], v[28:29] op_sel:[0,1,0] op_sel_hi:[1,1,0]\n"
      "v_pk_fma_f32 v[228:229], v[228:229], v[28:29], v[28:29] op_sel:[0,1,0] op_sel_hi:[1,1,0]\n"
      "v_pk_fma_f32 v[230:231], v[230:231], v[28:29], v[28:29] op_sel:[0,1,0] op_sel_hi:[1,1,0]\n"
      "v_pk_mul_f32 v[224:225], v[224:225], v[104:105]\n"
      "v_pk_mul_f32 v[226:227], v[226:227], v[106:107]\n"
      "v_pk_mul_f32 v[228:229], v[228:229], v[108:109]\n"
      "v_pk_mul_f32 v[230:231], v[230:231], v[110:111]\n"
      "v_exp_f32 v224, v224\n"
      "buffer_load_dwordx4 v23, s[48:51], s18 offen offset:3072 lds\n"
      "v_exp_f32 v225, v225\n"
      "v_exp_f32 v226, v226\n"
      "v_exp_f32 v227, v227\n"
      "v_exp_f32 v228, v228\n"
      "v_exp_f32 v229, v229\n"
      "v_exp_f32 v230, v230\n"
      "v_exp_f32 v231, v231\n"
      "v_pk_fma_f32 v[224:225], v[224:225], v[30:31], v[30:31] op_sel_hi:[1,0,0]\n"
      "v_pk_fma_f32 v[226:227], v[226:227], v[30:31], v[30:31] op_sel_hi:[1,0,0]\n"
      "v_pk_fma_f32 v[228:229], v[228:229], v[30:31], v[30:31] op_sel_hi:[1,0,0]\n"
      "v_pk_fma_f32 v[230:231], v[230:231], v[30:31], v[30:31] op_sel_hi:[1,0,0]\n"
      "v_rcp_f32 v224, v224\n"
      "v_rcp_f32 v225, v225\n"
      "v_rcp_f32 v226, v226\n"
      "v_rcp_f32 v227, v227\n"
      "v_rcp_f32 v228, v228\n"
      "v_rcp_f32 v229, v229\n"
      "v_rcp_f32 v230, v230\n"
      "v_rcp_f32 v231, v231\n"
      "v_pk_mul_f32 v[224:225], v[224:225], v[104:105]\n"
      "v_pk_mul_f32 v[226:227], v[226:227], v[106:107]\n"
      "v_pk_mul_f32 v[228:229], v[228:229], v[108:109]\n"
      "v_pk_mul_f32 v[230:231], v[230:231], v[110:111]\n"
      "v_cvt_pk_bf16_f32 v236, v224, v225\n"
      "v_cvt_pk_bf16_f32 v237, v226, v227\n"
      "v_cvt_pk_bf16_f32 v238, v228, v229\n"
      "v_cvt_pk_bf16_f32 v239, v230, v231\n"
      "buffer_store_dwordx4 v[236:239], v174, s[56:59], s23 offen offset:1024 sc1\n"
      "v_pk_mul_f32 v[224:225], v[112:113], v[112:113]\n"
      "v_pk_mul_f32 v[226:227], v[114:115], v[114:115]\n"
      "v_pk_mul_f32 v[228:229], v[116:117], v[116:117]\n"
      "v_pk_mul_f32 v[230:231], v[118:119], v[118:119]\n"
      "v_pk_fma_f32 v[224:225], v[224:225], v[28:29], v[28:29] op_sel:[0,1,0] op_sel_hi:[1,1,0]\n"
      "v_pk_fma_f32 v[226:227], v[226:227], v[28:29], v[28:29] op_sel:[0,1,0] op_sel_hi:[1,1,0]\n"
      "v_pk_fma_f32 v[228:229], v[228:229], v[28:29], v[28:29] op_sel:[0,1,0] op_sel_hi:[1,1,0]\n"
      "v_pk_fma_f32 v[230:231], v[230:231], v[28:29], v[28:29] op_sel:[0,1,0] op_sel_hi:[1,1,0]\n"
      "v_pk_mul_f32 v[224:225], v[224:225], v[112:113]\n"
      "v_pk_mul_f32 v[226:227], v[226:227], v[114:115]\n"
      "v_pk_mul_f32 v[228:229], v[228:229], v[116:117]\n"
      "v_pk_mul_f32 v[230:231], v[230:231], v[118:119]\n"
      "v_exp_f32 v224, v224\n"
      "v_exp_f32 v225, v225\n"
      "v_exp_f32 v226, v226\n"
      "v_exp_f32 v227, v227\n"
      "v_exp_f32 v228, v228\n"
      "v_exp_f32 v229, v229\n"
      "v_exp_f32 v230, v230\n"
      "v_exp_f32 v231, v231\n"
      "v_pk_fma_f32 v[224:225], v[224:225], v[30:31], v[30:31] op_sel_hi:[1,0,0]\n"
      "v_pk_fma_f32 v[226:227], v[226:227], v[30:31], v[30:31] op_sel_hi:[1,0,0]\n"
      "v_pk_fma_f32 v[228:229], v[228:229], v[30:31], v[30:31] op_sel_hi:[1,0,0]\n"
      "v_pk_fma_f32 v[230:231], v[230:231], v[30:31], v[30:31] op_sel_hi:[1,0,0]\n"
      "v_rcp_f32 v224, v224\n"
      "v_rcp_f32 v225, v225\n"
      "v_rcp_f32 v226, v226\n"
      "v_rcp_f32 v227, v227\n"
      "v_rcp_f32 v228, v228\n"
      "v_rcp_f32 v229, v229\n"
      "v_rcp_f32 v230, v230\n"
      "v_rcp_f32 v231, v231\n"
      "v_pk_mul_f32 v[224:225], v[224:225], v[112:113]\n"
      "v_pk_mul_f32 v[226:227], v[226:227], v[114:115]\n"
      "v_pk_mul_f32 v[228:229], v[228:229], v[116:117]\n"
      "v_pk_mul_f32 v[230:231], v[230:231], v[118:119]\n"
      "v_cvt_pk_bf16_f32 v232, v224, v225\n"
      "v_cvt_pk_bf16_f32 v233, v226, v227\n"
      "v_cvt_pk_bf16_f32 v234, v228, v229\n"
      "v_cvt_pk_bf16_f32 v235, v230, v231\n"
      "buffer_store_dwordx4 v[232:235], v174, s[56:59], s23 offen offset:2048 sc1\n"
      "s_mov_b64 exec, 0x3\n"
      "buffer_load_dwordx2 v[24:25], v201, s[56:59], s81 offen sc1\n"
      "s_mov_b64 exec, -1\n"
      "v_pk_mul_f32 v[224:225], v[120:121], v[120:121]\n"
      "v_pk_mul_f32 v[226:227], v[122:123], v[122:123]\n"
      "v_pk_mul_f32 v[228:229], v[124:125], v[124:125]\n"
      "v_pk_mul_f32 v[230:231], v[126:127], v[126:127]\n"
      "v_pk_fma_f32 v[224:225], v[224:225], v[28:29], v[28:29] op_sel:[0,1,0] op_sel_hi:[1,1,0]\n"
      "v_pk_fma_f32 v[226:227], v[226:227], v[28:29], v[28:29] op_sel:[0,1,0] op_sel_hi:[1,1,0]\n"
      "v_pk_fma_f32 v[228:229], v[228:229], v[28:29], v[28:29] op_sel:[0,1,0] op_sel_hi:[1,1,0]\n"
      "v_pk_fma_f32 v[230:231], v[230:231], v[28:29], v[28:29] op_sel:[0,1,0] op_sel_hi:[1,1,0]\n"
      "v_pk_mul_f32 v[224:225], v[224:225], v[120:121]\n"
      "v_pk_mul_f32 v[226:227], v[226:227], v[122:123]\n"
      "v_pk_mul_f32 v[228:229], v[228:229], v[124:125]\n"
      "v_pk_mul_f32 v[230:231], v[230:231], v[126:127]\n"
      "v_exp_f32 v224, v224\n"
      "v_exp_f32 v225, v225\n"
      "v_exp_f32 v226, v226\n"
      "v_exp_f32 v227, v227\n"
      "v_exp_f32 v228, v228\n"
      "v_exp_f32 v229, v229\n"
      "v_exp_f32 v230, v230\n"
      "v_exp_f32 v231, v231\n"
      "v_pk_fma_f32 v[224:225], v[224:225], v[30:31], v[30:31] op_sel_hi:[1,0,0]\n"
      "v_pk_fma_f32 v[226:227], v[226:227], v[30:31], v[30:31] op_sel_hi:[1,0,0]\n"
      "v_pk_fma_f32 v[228:229], v[228:229], v[30:31], v[30:31] op_sel_hi:[1,0,0]\n"
      "v_pk_fma_f32 v[230:231], v[230:231], v[30:31], v[30:31] op_sel_hi:[1,0,0]\n"
      "v_rcp_f32 v224, v224\n"
      "v_rcp_f32 v225, v225\n"
      "v_rcp_f32 v226, v226\n"
      "v_rcp_f32 v227, v227\n"
      "v_rcp_f32 v228, v228\n"
      "v_rcp_f32 v229, v229\n"
      "v_rcp_f32 v230, v230\n"
      "v_rcp_f32 v231, v231\n"
      "v_pk_mul_f32 v[224:225], v[224:225], v[120:121]\n"
      "v_pk_mul_f32 v[226:227], v[226:227], v[122:123]\n"
      "v_pk_mul_f32 v[228:229], v[228:229], v[124:125]\n"
      "v_pk_mul_f32 v[230:231], v[230:231], v[126:127]\n"
      "v_cvt_pk_bf16_f32 v236, v224, v225\n"
      "v_cvt_pk_bf16_f32 v237, v226, v227\n"
      "v_cvt_pk_bf16_f32 v238, v228, v229\n"
      "v_cvt_pk_bf16_f32 v239, v230, v231\n"
      "buffer_store_dwordx4 v[236:239], v174, s[56:59], s23 offen offset:3072 sc1\n"
      "s_waitcnt lgkmcnt(0)\n"
      "v_mfma_f32_16x16x32_bf16 v[240:243], v[196:199], a[192:195], 0\n"
      "v_mfma_f32_16x16x32_bf16 v[240:243], v[196:199], a[196:199], v[240:243]\n"
      "v_mfma_f32_16x16x32_bf16 v[240:243], v[196:199], a[200:203], v[240:243]\n"
      "ds_read_b64_tr_b16 a[216:217], v191 offset:4096\n"
      "ds_read_b64_tr_b16 a[218:219], v191 offset:5120\n"
      "v_mfma_f32_16x16x32_bf16 v[240:243], v[196:199], a[204:207], v[240:243]\n"
      "ds_read_b64_tr_b16 a[220:221], v191 offset:6144\n"
      "ds_read_b64_tr_b16 a[222:223], v191 offset:7168\n"
      "buffer_load_dwordx4 v[80:83], v5, s[24:27], s72 offen\n"
      "v_mfma_f32_16x16x32_bf16 v[240:243], v[196:199], a[208:211], v[240:243]\n"
      "v_mfma_f32_16x16x32_bf16 v[240:243], v[196:199], a[212:215], v[240:243]\n"
      "s_waitcnt vmcnt(18) lgkmcnt(0)\n"
      "s_barrier\n"
      "ds_read_b64_tr_b16 a[192:193], v191 offset:32768\n"
      "ds_read_b64_tr_b16 a[194:195], v191 offset:33792\n"
      "buffer_load_dwordx4 v[84:87], v5, s[24:27], s85 offen\n"
      "v_mfma_f32_16x16x32_bf16 v[240:243], v[196:199], a[216:219], v[240:243]\n"
      "ds_read_b64_tr_b16 a[196:197], v191 offset:34816\n"
      "ds_read_b64_tr_b16 a[198:199], v191 offset:35840\n"
      "ds_read_b64_tr_b16 a[200:201], v191 offset:36864\n"
      "ds_read_b64_tr_b16 a[202:203], v191 offset:37888\n"
      "v_mfma_f32_16x16x32_bf16 v[240:243], v[196:199], a[220:223], v[240:243]\n"
      "ds_read_b64_tr_b16 a[204:205], v191 offset:38912\n"
      "ds_read_b64_tr_b16 a[206:207], v191 offset:39936\n"
      "s_waitcnt lgkmcnt(6)\n"
      "v_mfma_f32_16x16x32_bf16 v[240:243], v[196:199], a[192:195], v[240:243]\n"
      "ds_read_b64_tr_b16 a[208:209], v192 offset:32768\n"
      "ds_read_b64_tr_b16 a[210:211], v192 offset:33792\n"
      "s_waitcnt lgkmcnt(6)\n"
      "v_mfma_f32_16x16x32_bf16 v[240:243], v[196:199], a[196:199], v[240:243]\n"
      "ds_read_b64_tr_b16 a[212:213], v192 offset:34816\n"
      "ds_read_b64_tr_b16 a[214:215], v192 offset:35840\n"
      "s_waitcnt lgkmcnt(6)\n"
      "v_mfma_f32_16x16x32_bf16 v[240:243], v[196:199], a[200:203], v[240:243]\n"
      "ds_read_b64_tr_b16 a[216:217], v192 offset:36864\n"
      "ds_read_b64_tr_b16 a[218:219], v192 offset:37888\n"
      "s_waitcnt lgkmcnt(6)\n"
      "v_mfma_f32_16x16x32_bf16 v[240:243], v[196:199], a[204:207], v[240:243]\n"
      "ds_read_b64_tr_b16 a[220:221], v192 offset:38912\n"
      "ds_read_b64_tr_b16 a[222:223], v192 offset:39936\n"
      "s_waitcnt lgkmcnt(6)\n"
      "v_mfma_f32_16x16x32_bf16 v[240:243], v[196:199], a[208:211], v[240:243]\n"
      "s_waitcnt lgkmcnt(4)\n"
      "v_mfma_f32_16x16x32_bf16 v[240:243], v[196:199], a[212:215], v[240:243]\n"
      "ds_read_b64_tr_b16 a[192:193], v16 offset:16384\n"
      "ds_read_b64_tr_b16 a[194:195], v16 offset:20480\n"
      "ds_read_b64_tr_b16 a[196:197], v19 offset:16384\n"
      "ds_read_b64_tr_b16 a[198:199], v19 offset:20480\n"
      "s_mov_b64 exec, 0x3\n"
      "s_waitcnt vmcnt(3)\n"
      "v_cmp_eq_u64 vcc, s[60:61], v[24:25]\n"
      "s_nop 0\n"
      "s_cmp_eq_u64 vcc, exec\n"
      "s_cbranch_scc0 L_slow_t\n"
      "L_ok_t:\n"
      "s_mov_b64 exec, -1\n"
      "buffer_load_dwordx4 a[224:227], v200, s[56:59], 0 offen sc1\n"
      "buffer_load_dwordx4 a[228:231], v203, s[56:59], 0 offen sc1\n"
      "s_waitcnt lgkmcnt(0)\n"
      "v_mfma_f32_16x16x32_bf16 v[240:243], v[196:199], a[216:219], v[240:243]\n"
      "ds_read_b64_tr_b16 a[200:201], v16 offset:24576\n"
      "ds_read_b64_tr_b16 a[202:203], v16 offset:28672\n"
      "v_mfma_f32_16x16x32_bf16 v[240:243], v[196:199], a[220:223], v[240:243]\n"
      "ds_read_b64_tr_b16 a[204:205], v19 offset:24576\n"
      "ds_read_b64_tr_b16 a[206:207], v19 offset:28672\n"
      "v_mfma_f32_32x32x16_bf16 v[128:143], a[192:195], a[0:3], 0\n"
      "ds_read_b64_tr_b16 a[208:209], v16 offset:17408\n"
      "ds_read_b64_tr_b16 a[210:211], v16 offset:21504\n"
      "ds_read_b64_tr_b16 a[212:213], v19 offset:17408\n"
      "ds_read_b64_tr_b16 a[214:215], v19 offset:21504\n"
      "ds_read_b64_tr_b16 a[128:129], v10 offset:49152\n"
      "v_mfma_f32_32x32x16_bf16 v[144:159], a[196:199], a[0:3], 0\n"
      "ds_read_b64_tr_b16 a[130:131], v10 offset:53248\n"
      "ds_read_b64_tr_b16 a[132:133], v13 offset:49152\n"
      "ds_read_b64_tr_b16 a[216:217], v16 offset:25600\n"
      "ds_read_b64_tr_b16 a[218:219], v16 offset:29696\n"
      "ds_read_b64_tr_b16 a[134:135], v13 offset:53248\n"
      "s_waitcnt lgkmcnt(12)\n"
      "v_mfma_f32_32x32x16_bf16 v[128:143], a[200:203], a[4:7], v[128:143]\n"
      "ds_read_b64_tr_b16 a[136:137], v10 offset:50176\n"
      "ds_read_b64_tr_b16 a[138:139], v10 offset:54272\n"
      "ds_read_b64_tr_b16 a[220:221], v19 offset:25600\n"
      "ds_read_b64_tr_b16 a[222:223], v19 offset:29696\n"
      "ds_read_b64_tr_b16 a[140:141], v13 offset:50176\n"
      "s_waitcnt lgkmcnt(14)\n"
      "v_mfma_f32_32x32x16_bf16 v[144:159], a[204:207], a[4:7], v[144:159]\n"
      "ds_read_b64_tr_b16 a[142:143], v13 offset:54272\n"
      "ds_read_b64_tr_b16 a[192:193], v16 offset:18432\n"
      "ds_read_b64_tr_b16 a[194:195], v16 offset:22528\n"
      "ds_read_b64_tr_b16 a[144:145], v10 offset:51200\n"
      "ds_read_b64_tr_b16 a[146:147], v10 offset:55296\n"
      "ds_write_b32 v195, v240 offset:512\n"
      "s_waitcnt lgkmcnt(14)\n"
      "v_mfma_f32_32x32x16_bf16 v[128:143], a[208:211], a[8:11], v[128:143]\n"
      "ds_read_b64_tr_b16 a[148:149], v13 offset:51200\n"
      "ds_read_b64_tr_b16 a[196:197], v19 offset:18432\n"
      "ds_read_b64_tr_b16 a[198:199], v19 offset:22528\n"
      "ds_read_b64_tr_b16 a[150:151], v13 offset:55296\n"
      "ds_read_b64_tr_b16 a[152:153], v10 offset:52224\n"
      "v_mfma_f32_32x32x16_bf16 v[144:159], a[212:215], a[8:11], v[144:159]\n"
      "ds_read_b64_tr_b16 a[154:155], v10 offset:56320\n"
      "ds_read_b64_tr_b16 a[200:201], v16 offset:26624\n"
      "ds_read_b64_tr_b16 a[202:203], v16 offset:30720\n"
      "ds_read_b64_tr_b16 a[156:157], v13 offset:52224\n"
      "ds_read_b64_tr_b16 a[158:159], v13 offset:56320\n"
      "s_waitcnt lgkmcnt(14)\n"
      "v_mfma_f32_32x32x16_bf16 v[128:143], a[216:219], a[12:15], v[128:143]\n"
      "s_waitcnt vmcnt(4)\n"
      "s_mov_b64 exec, 1\n"
      "s_xor_b32 s86, s63, 0xa0000\n"
      "buffer_store_dwordx2 v[26:27], v1, s[56:59], s86 offen sc1\n"
      "s_mov_b64 exec, -1\n"
      "ds_read_b64_tr_b16 a[160:161], v9 offset:16384\n"
      "v_mfma_f32_32x32x16_bf16 v[144:159], a[220:223], a[12:15], v[144:159]\n"
      "ds_read_b64_tr_b16 a[204:205], v19 offset:26624\n"
      "ds_read_b64_tr_b16 a[206:207], v19 offset:30720\n"
      "ds_read_b64_tr_b16 a[162:163], v9 offset:20480\n"
      "ds_read_b64_tr_b16 a[164:165], v12 offset:16384\n"
      "ds_read_b64_tr_b16 a[166:167], v12 offset:20480\n"
      "s_waitcnt lgkmcnt(13)\n"
      "v_mfma_f32_32x32x16_bf16 v[128:143], a[192:195], a[16:19], v[128:143]\n"
      "ds_read_b64_tr_b16 a[208:209], v16 offset:19456\n"
      "ds_read_b64_tr_b16 a[210:211], v16 offset:23552\n"
      "ds_read_b64_tr_b16 a[168:169], v9 offset:17408\n"
      "ds_read_b64_tr_b16 a[170:171], v9 offset:21504\n"
      "ds_read_b64_tr_b16 a[172:173], v12 offset:17408\n"
      "v_mfma_f32_32x32x16_bf16 v[144:159], a[196:199], a[16:19], v[144:159]\n"
      "ds_read_b64_tr_b16 a[212:213], v19 offset:19456\n"
      "ds_read_b64_tr_b16 a[214:215], v19 offset:23552\n"
      "ds_read_b64_tr_b16 a[174:175], v12 offset:21504\n"
      "ds_read_b64_tr_b16 a[176:177], v9 offset:18432\n"
      "ds_read_b64_tr_b16 a[178:179], v9 offset:22528\n"
      "s_waitcnt lgkmcnt(14)\n"
      "v_mfma_f32_32x32x16_bf16 v[128:143], a[200:203], a[20:23], v[128:143]\n"
      "ds_read_b64_tr_b16 a[216:217], v16 offset:27648\n"
      "ds_read_b64_tr_b16 a[218:219], v16 offset:31744\n"
      "ds_read_b64_tr_b16 a[180:181], v12 offset:18432\n"
      "ds_read_b64_tr_b16 a[182:183], v12 offset:22528\n"
      "ds_read_b64_tr_b16 a[184:185], v9 offset:19456\n"
      "s_waitcnt lgkmcnt(14)\n"
      "v_mfma_f32_32x32x16_bf16 v[144:159], a[204:207], a[20:23], v[144:159]\n"
      "ds_read_b64_tr_b16 a[220:221], v19 offset:27648\n"
      "ds_read_b64_tr_b16 a[222:223], v19 offset:31744\n"
      "ds_read_b64_tr_b16 a[186:187], v9 offset:23552\n"
      "ds_read_b64_tr_b16 a[188:189], v12 offset:19456\n"
      "ds_read_b64_tr_b16 a[190:191], v12 offset:23552\n"
      "s_waitcnt lgkmcnt(14)\n"
      "v_mfma_f32_32x32x16_bf16 v[128:143], a[208:211], a[24:27], v[128:143]\n"
      "s_waitcnt lgkmcnt(13)\n"
      "v_mfma_f32_32x32x16_bf16 v[144:159], a[212:215], a[24:27], v[144:159]\n"
      "s_waitcnt lgkmcnt(0)\n"
      "s_barrier\n"
      "ds_read_b64_tr_b16 a[192:193], v15 offset:0\n"
      "ds_read_b64_tr_b16 a[194:195], v15 offset:4096\n"
      "s_add_u32 m0, s15, 0x14000\n"
      "v_add_u32 v23, 0x20000, v23\n"
      "buffer_load_dwordx4 v23, s[48:51], 0 offen offset:0 lds\n"
      "v_mfma_f32_32x32x16_bf16 v[128:143], a[216:219], a[28:31], v[128:143]\n"
      "ds_read_b64_tr_b16 a[196:197], v18 offset:0\n"
      "ds_read_b64_tr_b16 a[198:199], v18 offset:4096\n"
      "ds_read_b64_tr_b16 a[200:201], v15 offset:8192\n"
      "ds_read_b64_tr_b16 a[202:203], v15 offset:12288\n"
      "v_mfma_f32_32x32x16_bf16 v[144:159], a[220:223], a[28:31], v[144:159]\n"
      "buffer_load_dwordx4 v23, s[48:51], s16 offen offset:1024 lds\n"
      "s_waitcnt lgkmcnt(4)\n"
      "v_mfma_f32_32x32x16_bf16 v[128:143], a[192:195], a[32:35], v[128:143]\n"
      "ds_read_b64_tr_b16 a[204:205], v18 offset:8192\n"
      "ds_read_b64_tr_b16 a[206:207], v18 offset:12288\n"
      "ds_read_b64_tr_b16 a[208:209], v15 offset:1024\n"
      "ds_read_b64_tr_b16 a[210:211], v15 offset:5120\n"
      "s_waitcnt lgkmcnt(6)\n"
      "v_mfma_f32_32x32x16_bf16 v[144:159], a[196:199], a[32:35], v[144:159]\n"
      "buffer_load_dwordx4 v23, s[48:51], s17 offen offset:2048 lds\n"
      "s_waitcnt lgkmcnt(4)\n"
      "v_mfma_f32_32x32x16_bf16 v[128:143], a[200:203], a[36:39], v[128:143]\n"
      "ds_read_b64_tr_b16 a[212:213], v18 offset:1024\n"
      "ds_read_b64_tr_b16 a[214:215], v18 offset:5120\n"
      "ds_read_b64_tr_b16 a[216:217], v15 offset:9216\n"
      "ds_read_b64_tr_b16 a[218:219], v15 offset:13312\n"
      "s_waitcnt lgkmcnt(6)\n"
      "v_mfma_f32_32x32x16_bf16 v[144:159], a[204:207], a[36:39], v[144:159]\n"
      "buffer_load_dwordx4 v23, s[48:51], s18 offen offset:3072 lds\n"
      "s_waitcnt lgkmcnt(4)\n"
      "v_mfma_f32_32x32x16_bf16 v[128:143], a[208:211], a[40:43], v[128:143]\n"
      "ds_read_b64_tr_b16 a[220:221], v18 offset:9216\n"
      "ds_read_b64_tr_b16 a[222:223], v18 offset:13312\n"
      "ds_read_b64_tr_b16 a[192:193], v15 offset:2048\n"
      "ds_read_b64_tr_b16 a[194:195], v15 offset:6144\n"
      "s_waitcnt vmcnt(6)\n"
      "v_mfma_f32_32x32x16_bf16 v[96:111], a[224:227], v[88:91], 0\n"
      "s_add_u32 m0, s15, 0x1c000\n"
      "v_add_u32 v23, 0x20000, v23\n"
      "buffer_load_dwordx4 v23, s[48:51], 0 offen offset:0 lds\n"
      "s_waitcnt lgkmcnt(4)\n"
      "v_mfma_f32_32x32x16_bf16 v[144:159], a[212:215], a[40:43], v[144:159]\n"
      "ds_read_b64_tr_b16 a[196:197], v18 offset:2048\n"
      "ds_read_b64_tr_b16 a[198:199], v18 offset:6144\n"
      "s_waitcnt vmcnt(6)\n"
      "v_mfma_f32_32x32x16_bf16 v[112:127], a[228:231], v[92:95], 0\n"
      "buffer_load_dwordx4 v23, s[48:51], s16 offen offset:1024 lds\n"
      "s_add_u32 s22, s74, 0x800000\n"
      "v_mfma_f32_32x32x16_bf16 v[128:143], a[216:219], a[44:47], v[128:143]\n"
      "ds_read_b64_tr_b16 a[200:201], v15 offset:10240\n"
      "ds_read_b64_tr_b16 a[202:203], v15 offset:14336\n"
      "v_add_f32 v104, s68, v104\n"
      "ds_read_b64_tr_b16 a[204:205], v18 offset:10240\n"
      "ds_read_b64_tr_b16 a[206:207], v18 offset:14336\n"
      "s_waitcnt lgkmcnt(6)\n"
      "v_mfma_f32_32x32x16_bf16 v[144:159], a[220:223], a[44:47], v[144:159]\n"
      "buffer_load_dwordx4 v23, s[48:51], s17 offen offset:2048 lds\n"
      "v_rcp_f32 v104, v104\n"
      "v_add_f32 v120, s68, v120\n"
      "v_rcp_f32 v120, v120\n"
      "v_mfma_f32_32x32x16_bf16 v[128:143], a[192:195], a[48:51], v[128:143]\n"
      "ds_read_b64_tr_b16 a[208:209], v15 offset:3072\n"
      "ds_read_b64_tr_b16 a[210:211], v15 offset:7168\n"
      "v_mul_f32 v96, v96, v104\n"
      "v_mul_f32 v97, v97, v104\n"
      "v_mul_f32 v98, v98, v104\n"
      "s_waitcnt lgkmcnt(4)\n"
      "v_mfma_f32_32x32x16_bf16 v[144:159], a[196:199], a[48:51], v[144:159]\n"
      "buffer_load_dwordx4 v23, s[48:51], s18 offen offset:3072 lds\n"
      "v_mul_f32 v99, v99, v104\n"
      "v_mul_f32 v100, v100, v104\n"
      "v_mul_f32 v101, v101, v104\n"
      "v_mfma_f32_32x32x16_bf16 v[128:143], a[200:203], a[52:55], v[128:143]\n"
      "ds_read_b64_tr_b16 a[212:213], v18 offset:3072\n"
      "ds_read_b64_tr_b16 a[214:215], v18 offset:7168\n"
      "ds_read_b64_tr_b16 a[216:217], v15 offset:11264\n"
      "ds_read_b64_tr_b16 a[218:219], v15 offset:15360\n"
      "v_mul_f32 v102, v102, v104\n"
      "v_mul_f32 v103, v103, v104\n"
      "v_cvt_pk_bf16_f32 v88, v96, v97\n"
      "s_waitcnt lgkmcnt(6)\n"
      "v_mfma_f32_32x32x16_bf16 v[144:159], a[204:207], a[52:55], v[144:159]\n"
      "s_add_u32 m0, s15, 0x4000\n"
      "v_add_u32 v175, 0xfffd0000, v175\n"
      "buffer_load_dwordx4 v175, s[32:35], 0 offen offset:0 lds\n"
      "v_cvt_pk_bf16_f32 v89, v98, v99\n"
      "v_cvt_pk_bf16_f32 v90, v100, v101\n"
      "v_cvt_pk_bf16_f32 v91, v102, v103\n"
      "s_waitcnt lgkmcnt(4)\n"
      "v_mfma_f32_32x32x16_bf16 v[128:143], a[208:211], a[56:59], v[128:143]\n"
      "ds_read_b64_tr_b16 a[220:221], v18 offset:11264\n"
      "ds_read_b64_tr_b16 a[222:223], v18 offset:15360\n"
      "buffer_store_dwordx4 v[88:91], v174, s[56:59], s22 offen offset:0 sc1\n"
      "v_mul_f32 v112, v112, v120\n"
      "v_mul_f32 v113, v113, v120\n"
      "s_waitcnt lgkmcnt(2)\n"
      "v_mfma_f32_32x32x16_bf16 v[144:159], a[212:215], a[56:59], v[144:159]\n"
      "s_waitcnt lgkmcnt(0)\n"
      "s_barrier\n"
      "ds_read_b64_tr_b16 a[192:193], v15 offset:32768\n"
      "ds_read_b64_tr_b16 a[194:195], v15 offset:36864\n"
      "buffer_load_dwordx4 v175, s[32:35], s16 offen offset:1024 lds\n"
      "v_mul_f32 v114, v114, v120\n"
      "v_mul_f32 v115, v115, v120\n"
      "v_mul_f32 v116, v116, v120\n"
      "v_mfma_f32_32x32x16_bf16 v[128:143], a[216:219], a[60:63], v[128:143]\n"
      "ds_read_b64_tr_b16 a[196:197], v18 offset:32768\n"
      "ds_read_b64_tr_b16 a[198:199], v18 offset:36864\n"
      "ds_read_b64_tr_b16 a[200:201], v15 offset:40960\n"
      "ds_read_b64_tr_b16 a[202:203], v15 offset:45056\n"
      "v_mul_f32 v117, v117, v120\n"
      "v_mul_f32 v118, v118, v120\n"
      "v_mul_f32 v119, v119, v120\n"
      "v_mfma_f32_32x32x16_bf16 v[144:159], a[220:223], a[60:63], v[144:159]\n"
      "buffer_load_dwordx4 v175, s[32:35], s17 offen offset:2048 lds\n"
      "v_cvt_pk_bf16_f32 v92, v112, v113\n"
      "v_cvt_pk_bf16_f32 v93, v114, v115\n"
      "v_cvt_pk_bf16_f32 v94, v116, v117\n"
      "v_cvt_pk_bf16_f32 v95, v118, v119\n"
      "buffer_store_dwordx4 v[92:95], v174, s[56:59], s22 offen offset:1024 sc1\n"
      "s_waitcnt lgkmcnt(0)\n"
      "v_mfma_f32_32x32x16_bf16 v[128:143], a[192:195], a[64:67], v[128:143]\n"
      "ds_read_b64_tr_b16 a[204:205], v18 offset:40960\n"
      "ds_read_b64_tr_b16 a[206:207], v18 offset:45056\n"
      "ds_read_b64_tr_b16 a[208:209], v15 offset:33792\n"
      "ds_read_b64_tr_b16 a[210:211], v15 offset:37888\n"
      "ds_read_b64_tr_b16 a[0:1], v9 offset:49152\n"
      "v_mfma_f32_32x32x16_bf16 v[144:159], a[196:199], a[64:67], v[144:159]\n"
      "buffer_load_dwordx4 v175, s[32:35], s18 offen offset:3072 lds\n"
      "ds_read_b64_tr_b16 a[2:3], v9 offset:53248\n"
      "ds_read_b64_tr_b16 a[4:5], v12 offset:49152\n"
      "v_mfma_f32_32x32x16_bf16 v[128:143], a[200:203], a[68:71], v[128:143]\n"
      "ds_read_b64_tr_b16 a[212:213], v18 offset:33792\n"
      "ds_read_b64_tr_b16 a[214:215], v18 offset:37888\n"
      "ds_read_b64_tr_b16 a[216:217], v15 offset:41984\n"
      "ds_read_b64_tr_b16 a[218:219], v15 offset:46080\n"
      "ds_read_b64_tr_b16 a[6:7], v12 offset:53248\n"
      "ds_read_b64_tr_b16 a[8:9], v9 offset:50176\n"
      "ds_read_b64_tr_b16 a[10:11], v9 offset:54272\n"
      "s_waitcnt lgkmcnt(12)\n"
      "v_mfma_f32_32x32x16_bf16 v[144:159], a[204:207], a[68:71], v[144:159]\n"
      "s_add_u32 m0, s15, 0x0\n"
      "v_add_u32 v175, 0x10000, v175\n"
      "buffer_load_dwordx4 v175, s[32:35], 0 offen offset:0 lds\n"
      "ds_read_b64_tr_b16 a[12:13], v12 offset:50176\n"
      "ds_read_b64_tr_b16 a[14:15], v12 offset:54272\n"
      "s_waitcnt lgkmcnt(12)\n"
      "v_mfma_f32_32x32x16_bf16 v[128:143], a[208:211], a[72:75], v[128:143]\n"
      "ds_read_b64_tr_b16 a[220:221], v18 offset:41984\n"
      "ds_read_b64_tr_b16 a[222:223], v18 offset:46080\n"
      "ds_read_b64_tr_b16 a[192:193], v15 offset:34816\n"
      "ds_read_b64_tr_b16 a[194:195], v15 offset:38912\n"
      "ds_read_b64_tr_b16 a[16:17], v9 offset:51200\n"
      "ds_read_b64_tr_b16 a[18:19], v9 offset:55296\n"
      "ds_read_b64_tr_b16 a[20:21], v12 offset:51200\n"
      "s_waitcnt lgkmcnt(14)\n"
      "v_mfma_f32_32x32x16_bf16 v[144:159], a[212:215], a[72:75], v[144:159]\n"
      "buffer_load_dwordx4 v175, s[32:35], s16 offen offset:1024 lds\n"
      "ds_read_b64_tr_b16 a[22:23], v12 offset:55296\n"
      "ds_read_b64_tr_b16 a[24:25], v9 offset:52224\n"
      "ds_read_b64_tr_b16 a[26:27], v9 offset:56320\n"
      "s_waitcnt lgkmcnt(14)\n"
      "v_mfma_f32_32x32x16_bf16 v[128:143], a[216:219], a[76:79], v[128:143]\n"
      "ds_read_b64_tr_b16 a[196:197], v18 offset:34816\n"
      "ds_read_b64_tr_b16 a[198:199], v18 offset:38912\n"
      "ds_read_b64_tr_b16 a[200:201], v15 offset:43008\n"
      "ds_read_b64_tr_b16 a[202:203], v15 offset:47104\n"
      "ds_read_b64_tr_b16 a[28:29], v12 offset:52224\n"
      "ds_read_b64_tr_b16 a[30:31], v12 offset:56320\n"
      "ds_read_b64_tr_b16 a[32:33], v11 offset:0\n"
      "s_waitcnt lgkmcnt(14)\n"
      "v_mfma_f32_32x32x16_bf16 v[144:159], a[220:223], a[76:79], v[144:159]\n"
      "buffer_load_dwordx4 v175, s[32:35], s17 offen offset:2048 lds\n"
      "ds_read_b64_tr_b16 a[34:35], v11 offset:4096\n"
      "ds_read_b64_tr_b16 a[36:37], v14 offset:0\n"
      "s_waitcnt lgkmcnt(14)\n"
      "v_mfma_f32_32x32x16_bf16 v[128:143], a[192:195], a[80:83], v[128:143]\n"
      "ds_read_b64_tr_b16 a[204:205], v18 offset:43008\n"
      "ds_read_b64_tr_b16 a[206:207], v18 offset:47104\n"
      "ds_read_b64_tr_b16 a[208:209], v15 offset:35840\n"
      "ds_read_b64_tr_b16 a[210:211], v15 offset:39936\n"
      "ds_read_b64_tr_b16 a[38:39], v14 offset:4096\n"
      "ds_read_b64_tr_b16 a[40:41], v11 offset:1024\n"
      "ds_read_b64_tr_b16 a[42:43], v11 offset:5120\n"
      "s_waitcnt lgkmcnt(14)\n"
      "v_mfma_f32_32x32x16_bf16 v[144:159], a[196:199], a[80:83], v[144:159]\n"
      "buffer_load_dwordx4 v175, s[32:35], s18 offen offset:3072 lds\n"
      "ds_read_b64_tr_b16 a[44:45], v14 offset:1024\n"
      "ds_read_b64_tr_b16 a[46:47], v14 offset:5120\n"
      "ds_read_b64_tr_b16 a[48:49], v11 offset:2048\n"
      "s_waitcnt lgkmcnt(14)\n"
      "v_mfma_f32_32x32x16_bf16 v[128:143], a[200:203], a[84:87], v[128:143]\n"
      "ds_read_b64_tr_b16 a[212:213], v18 offset:35840\n"
      "ds_read_b64_tr_b16 a[214:215], v18 offset:39936\n"
      "ds_read_b64_tr_b16 a[216:217], v15 offset:44032\n"
      "ds_read_b64_tr_b16 a[218:219], v15 offset:48128\n"
      "ds_read_b64_tr_b16 a[50:51], v11 offset:6144\n"
      "ds_read_b64_tr_b16 a[52:53], v14 offset:2048\n"
      "ds_read_b64_tr_b16 a[54:55], v14 offset:6144\n"
      "s_waitcnt lgkmcnt(14)\n"
      "v_mfma_f32_32x32x16_bf16 v[144:159], a[204:207], a[84:87], v[144:159]\n"
      "ds_read_b64_tr_b16 a[220:221], v18 offset:44032\n"
      "ds_read_b64_tr_b16 a[222:223], v18 offset:48128\n"
      "ds_read_b64_tr_b16 a[56:57], v11 offset:3072\n"
      "ds_read_b64_tr_b16 a[58:59], v11 offset:7168\n"
      "ds_read_b64_tr_b16 a[60:61], v14 offset:3072\n"
      "ds_read_b64_tr_b16 a[62:63], v14 offset:7168\n"
      "s_waitcnt lgkmcnt(14)\n"
      "v_mfma_f32_32x32x16_bf16 v[128:143], a[208:211], a[88:91], v[128:143]\n"
      "ds_read_b32 v214, v194 offset:512\n"
      "ds_read_b32 v215, v194 offset:768\n"
      "ds_read_b32 v218, v194 offset:640\n"
      "ds_read_b32 v219, v194 offset:896\n"
      "s_waitcnt lgkmcnt(2)\n"
      "v_add_f32 v214, v214, v215\n"
      "v_mfma_f32_32x32x16_bf16 v[144:159], a[212:215], a[88:91], v[144:159]\n"
      "s_waitcnt lgkmcnt(0)\n"
      "s_barrier\n"
      "ds_read_b64_tr_b16 a[192:193], v16 offset:32768\n"
      "ds_read_b64_tr_b16 a[194:195], v16 offset:36864\n"
      "s_add_u32 m0, s15, 0x8000\n"
      "v_add_u32 v175, 0x10000, v175\n"
      "buffer_load_dwordx4 v175, s[32:35], 0 offen offset:0 lds\n"
      "v_mfma_f32_32x32x16_bf16 v[128:143], a[216:219], a[92:95], v[128:143]\n"
      "s_waitcnt vmcnt(6)\n"
      "s_mov_b64 exec, 1\n"
      "s_xor_b32 s86, s79, 0x60000\n"
      "buffer_store_dwordx2 v[26:27], v1, s[56:59], s86 offen sc1\n"
      "s_mov_b64 exec, -1\n"
      "ds_read_b64_tr_b16 a[196:197], v19 offset:32768\n"
      "ds_read_b64_tr_b16 a[198:199], v19 offset:36864\n"
      "ds_read_b64_tr_b16 a[200:201], v16 offset:40960\n"
      "ds_read_b64_tr_b16 a[202:203], v16 offset:45056\n"
      "v_mfma_f32_32x32x16_bf16 v[144:159], a[220:223], a[92:95], v[144:159]\n"
      "ds_read_b64_tr_b16 a[204:205], v19 offset:40960\n"
      "ds_read_b64_tr_b16 a[206:207], v19 offset:45056\n"
      "ds_read_b64_tr_b16 a[208:209], v16 offset:33792\n"
      "ds_read_b64_tr_b16 a[210:211], v16 offset:37888\n"
      "v_cvt_pk_bf16_f32 v213, v214, 0\n"
      "s_waitcnt lgkmcnt(8)\n"
      "v_mfma_f32_32x32x16_bf16 v[128:143], a[192:195], a[96:99], v[128:143]\n"
      "buffer_load_dwordx4 v175, s[32:35], s16 offen offset:1024 lds\n"
      "s_waitcnt lgkmcnt(6)\n"
      "v_mfma_f32_32x32x16_bf16 v[144:159], a[196:199], a[96:99], v[144:159]\n"
      "ds_read_b64_tr_b16 a[212:213], v19 offset:33792\n"
      "ds_read_b64_tr_b16 a[214:215], v19 offset:37888\n"
      "ds_read_b64_tr_b16 a[216:217], v16 offset:41984\n"
      "ds_read_b64_tr_b16 a[218:219], v16 offset:46080\n"
      "v_lshlrev_b32 v215, 16, v213\n"
      "s_waitcnt lgkmcnt(8)\n"
      "v_mfma_f32_32x32x16_bf16 v[128:143], a[200:203], a[100:103], v[128:143]\n"
      "buffer_load_dwordx4 v175, s[32:35], s17 offen offset:2048 lds\n"
      "s_mov_b64 exec, 0x7f\n"
      "buffer_load_dwordx2 v[24:25], v201, s[56:59], s83 offen sc1\n"
      "s_mov_b64 exec, -1\n"
      "s_waitcnt lgkmcnt(0)\n"
      "v_mfma_f32_32x32x16_bf16 v[144:159], a[204:207], a[100:103], v[144:159]\n"
      "ds_read_b64_tr_b16 a[220:221], v19 offset:41984\n"
      "ds_read_b64_tr_b16 a[222:223], v19 offset:46080\n"
      "ds_read_b64_tr_b16 a[192:193], v16 offset:34816\n"
      "ds_read_b64_tr_b16 a[194:195], v16 offset:38912\n"
      "v_sub_f32 v215, v214, v215\n"
      "v_mfma_f32_32x32x16_bf16 v[128:143], a[208:211], a[104:107], v[128:143]\n"
      "buffer_load_dwordx4 v175, s[32:35], s18 offen offset:3072 lds\n"
      "v_mfma_f32_32x32x16_bf16 v[144:159], a[212:215], a[104:107], v[144:159]\n"
      "ds_read_b64_tr_b16 a[196:197], v19 offset:34816\n"
      "ds_read_b64_tr_b16 a[198:199], v19 offset:38912\n"
      "ds_read_b64_tr_b16 a[200:201], v16 offset:43008\n"
      "ds_read_b64_tr_b16 a[202:203], v16 offset:47104\n"
      "v_cvt_pk_bf16_f32 v212, v214, v215\n"
      "v_mfma_f32_32x32x16_bf16 v[128:143], a[216:219], a[108:111], v[128:143]\n"
      "s_add_u32 m0, s15, 0xc000\n"
      "v_add_u32 v175, 0x10000, v175\n"
      "buffer_load_dwordx4 v175, s[32:35], 0 offen offset:0 lds\n"
      "s_waitcnt lgkmcnt(4)\n"
      "v_mfma_f32_32x32x16_bf16 v[144:159], a[220:223], a[108:111], v[144:159]\n"
      "ds_read_b64_tr_b16 a[204:205], v19 offset:43008\n"
      "ds_read_b64_tr_b16 a[206:207], v19 offset:47104\n"
      "ds_read_b64_tr_b16 a[208:209], v16 offset:35840\n"
      "ds_read_b64_tr_b16 a[210:211], v16 offset:39936\n"
      "v_mov_b32 v214, 0\n"
      "v_mfma_f32_32x32x16_bf16 v[128:143], a[192:195], a[112:115], v[128:143]\n"
      "buffer_load_dwordx4 v175, s[32:35], s16 offen offset:1024 lds\n"
      "s_waitcnt lgkmcnt(4)\n"
      "v_mfma_f32_32x32x16_bf16 v[144:159], a[196:199], a[112:115], v[144:159]\n"
      "ds_read_b64_tr_b16 a[212:213], v19 offset:35840\n"
      "ds_read_b64_tr_b16 a[214:215], v19 offset:39936\n"
      "v_mov_b32 v215, 0\n"
      "ds_read_b64_tr_b16 a[216:217], v16 offset:44032\n"
      "ds_read_b64_tr_b16 a[218:219], v16 offset:48128\n"
      "v_mfma_f32_32x32x16_bf16 v[128:143], a[200:203], a[116:119], v[128:143]\n"
      "buffer_load_dwordx4 v175, s[32:35], s17 offen offset:2048 lds\n"
      "v_add_f32 v218, v218, v219\n"
      "s_waitcnt lgkmcnt(4)\n"
      "v_mfma_f32_32x32x16_bf16 v[144:159], a[204:207], a[116:119], v[144:159]\n"
      "ds_read_b64_tr_b16 a[220:221], v19 offset:44032\n"
      "ds_read_b64_tr_b16 a[222:223], v19 offset:48128\n"
      "v_cvt_pk_bf16_f32 v217, v218, 0\n"
      "v_lshlrev_b32 v219, 16, v217\n"
      "v_sub_f32 v219, v218, v219\n"
      "v_mfma_f32_32x32x16_bf16 v[128:143], a[208:211], a[120:123], v[128:143]\n"
      "buffer_load_dwordx4 v175, s[32:35], s18 offen offset:3072 lds\n"
      "s_waitcnt lgkmcnt(2)\n"
      "v_mfma_f32_32x32x16_bf16 v[144:159], a[212:215], a[120:123], v[144:159]\n"
      "s_waitcnt lgkmcnt(0)\n"
      "s_barrier\n"
      "v_cvt_pk_bf16_f32 v216, v218, v219\n"
      "v_mov_b32 v218, 0\n"
      "v_mov_b32 v219, 0\n"
      "v_mfma_f32_32x32x16_bf16 v[128:143], a[216:219], a[124:127], v[128:143]\n"
      "s_add_u32 m0, s15, 0x20000\n"
      "v_add_u32 v23, 0xfff50000, v23\n"
      "buffer_load_dwordx4 v23, s[48:51], 0 offen offset:0 lds\n"
      "buffer_load_dwordx4 v23, s[48:51], s16 offen offset:1024 lds\n"
      "buffer_load_dwordx4 v23, s[48:51], s17 offen offset:2048 lds\n"
      "buffer_load_dwordx4 v23, s[48:51], s18 offen offset:3072 lds\n"
      "s_add_u32 m0, s15, 0x18000\n"
      "v_add_u32 v23, 0x20000, v23\n"
      "buffer_load_dwordx4 v23, s[48:51], 0 offen offset:0 lds\n"
      "buffer_load_dwordx4 v23, s[48:51], s16 offen offset:1024 lds\n"
      "s_mov_b64 exec, 0x7f\n"
      "s_waitcnt vmcnt(11)\n"
      "v_cmp_eq_u64 vcc, s[60:61], v[24:25]\n"
      "s_nop 0\n"
      "s_cmp_eq_u64 vcc, exec\n"
      "s_cbranch_scc0 L_slow_g\n"
      "L_ok_g:\n"
      "s_mov_b64 exec, -1\n"
      "v_mfma_f32_32x32x16_bf16 v[144:159], a[220:223], a[124:127], v[144:159]\n"
      "s_add_u32 s78, s75, 0xa00000\n"
      "buffer_load_dwordx4 a[64:67], v174, s[56:59], s78 offen offset:0 sc1\n"
      "v_mfma_f32_32x32x16_bf16 v[128:143], v[212:215], v[220:223], v[128:143]\n"
      "v_mfma_f32_32x32x16_bf16 v[144:159], v[216:219], v[220:223], v[144:159]\n"
      "s_add_u32 s23, s76, 0xa01000\n"
      "s_nop 9\n"
      "v_pk_mul_f32 v[224:225], v[128:129], v[128:129]\n"
      "buffer_load_dwordx4 a[68:71], v174, s[56:59], s78 offen offset:1024 sc1\n"
      "v_pk_mul_f32 v[226:227], v[130:131], v[130:131]\n"
      "v_pk_mul_f32 v[228:229], v[132:133], v[132:133]\n"
      "v_pk_mul_f32 v[230:231], v[134:135], v[134:135]\n"
      "v_pk_fma_f32 v[224:225], v[224:225], v[28:29], v[28:29] op_sel:[0,1,0] op_sel_hi:[1,1,0]\n"
      "v_pk_fma_f32 v[226:227], v[226:227], v[28:29], v[28:29] op_sel:[0,1,0] op_sel_hi:[1,1,0]\n"
      "v_pk_fma_f32 v[228:229], v[228:229], v[28:29], v[28:29] op_sel:[0,1,0] op_sel_hi:[1,1,0]\n"
      "v_pk_fma_f32 v[230:231], v[230:231], v[28:29], v[28:29] op_sel:[0,1,0] op_sel_hi:[1,1,0]\n"
      "v_pk_mul_f32 v[224:225], v[224:225], v[128:129]\n"
      "v_pk_mul_f32 v[226:227], v[226:227], v[130:131]\n"
      "v_pk_mul_f32 v[228:229], v[228:229], v[132:133]\n"
      "v_pk_mul_f32 v[230:231], v[230:231], v[134:135]\n"
      "v_exp_f32 v224, v224\n"
      "buffer_load_dwordx4 a[72:75], v174, s[56:59], s78 offen offset:2048 sc1\n"
      "v_exp_f32 v225, v225\n"
      "v_exp_f32 v226, v226\n"
      "v_exp_f32 v227, v227\n"
      "v_exp_f32 v228, v228\n"
      "v_exp_f32 v229, v229\n"
      "v_exp_f32 v230, v230\n"
      "v_exp_f32 v231, v231\n"
      "buffer_load_dwordx4 a[76:79], v174, s[56:59], s78 offen offset:3072 sc1\n"
      "v_pk_fma_f32 v[224:225], v[224:225], v[30:31], v[30:31] op_sel_hi:[1,0,0]\n"
      "v_pk_fma_f32 v[226:227], v[226:227], v[30:31], v[30:31] op_sel_hi:[1,0,0]\n"
      "v_pk_fma_f32 v[228:229], v[228:229], v[30:31], v[30:31] op_sel_hi:[1,0,0]\n"
      "v_pk_fma_f32 v[230:231], v[230:231], v[30:31], v[30:31] op_sel_hi:[1,0,0]\n"
      "v_rcp_f32 v224, v224\n"
      "v_rcp_f32 v225, v225\n"
      "v_rcp_f32 v226, v226\n"
      "v_rcp_f32 v227, v227\n"
      "v_rcp_f32 v228, v228\n"
      "s_add_u32 s78, s75, 0xa02000\n"
      "buffer_load_dwordx4 a[80:83], v174, s[56:59], s78 offen offset:0 sc1\n"
      "v_rcp_f32 v229, v229\n"
      "v_rcp_f32 v230, v230\n"
      "v_rcp_f32 v231, v231\n"
      "v_pk_mul_f32 v[224:225], v[224:225], v[128:129]\n"
      "v_pk_mul_f32 v[226:227], v[226:227], v[130:131]\n"
      "v_pk_mul_f32 v[228:229], v[228:229], v[132:133]\n"
      "v_pk_mul_f32 v[230:231], v[230:231], v[134:135]\n"
      "v_cvt_pk_bf16_f32 v232, v224, v225\n"
      "v_cvt_pk_bf16_f32 v233, v226, v227\n"
      "v_cvt_pk_bf16_f32 v234, v228, v229\n"
      "buffer_load_dwordx4 a[84:87], v174, s[56:59], s78 offen offset:1024 sc1\n"
      "v_cvt_pk_bf16_f32 v235, v230, v231\n"
      "buffer_store_dwordx4 v[232:235], v174, s[56:59], s23 offen offset:0 sc1\n"
      "v_pk_mul_f32 v[224:225], v[136:137], v[136:137]\n"
      "v_pk_mul_f32 v[226:227], v[138:139], v[138:139]\n"
      "v_pk_mul_f32 v[228:229], v[140:141], v[140:141]\n"
      "v_pk_mul_f32 v[230:231], v[142:143], v[142:143]\n"
      "v_pk_fma_f32 v[224:225], v[224:225], v[28:29], v[28:29] op_sel:[0,1,0] op_sel_hi:[1,1,0]\n"
      "v_pk_fma_f32 v[226:227], v[226:227], v[28:29], v[28:29] op_sel:[0,1,0] op_sel_hi:[1,1,0]\n"
      "v_pk_fma_f32 v[228:229], v[228:229], v[28:29], v[28:29] op_sel:[0,1,0] op_sel_hi:[1,1,0]\n"
      "v_pk_fma_f32 v[230:231], v[230:231], v[28:29], v[28:29] op_sel:[0,1,0] op_sel_hi:[1,1,0]\n"
      "v_pk_mul_f32 v[224:225], v[224:225], v[136:137]\n"
      "v_pk_mul_f32 v[226:227], v[226:227], v[138:139]\n"
      "v_pk_mul_f32 v[228:229], v[228:229], v[140:141]\n"
      "v_pk_mul_f32 v[230:231], v[230:231], v[142:143]\n"
      "v_exp_f32 v224, v224\n"
      "buffer_load_dwordx4 a[88:91], v174, s[56:59], s78 offen offset:2048 sc1\n"
      "v_exp_f32 v225, v225\n"
      "v_exp_f32 v226, v226\n"
      "v_exp_f32 v227, v227\n"
      "v_exp_f32 v228, v228\n"
      "v_exp_f32 v229, v229\n"
      "v_exp_f32 v230, v230\n"
      "v_exp_f32 v231, v231\n"
      "buffer_load_dwordx4 a[92:95], v174, s[56:59], s78 offen offset:3072 sc1\n"
      "v_pk_fma_f32 v[224:225], v[224:225], v[30:31], v[30:31] op_sel_hi:[1,0,0]\n"
      "v_pk_fma_f32 v[226:227], v[226:227], v[30:31], v[30:31] op_sel_hi:[1,0,0]\n"
      "v_pk_fma_f32 v[228:229], v[228:229], v[30:31], v[30:31] op_sel_hi:[1,0,0]\n"
      "v_pk_fma_f32 v[230:231], v[230:231], v[30:31], v[30:31] op_sel_hi:[1,0,0]\n"
      "v_rcp_f32 v224, v224\n"
      "v_rcp_f32 v225, v225\n"
      "v_rcp_f32 v226, v226\n"
      "v_rcp_f32 v227, v227\n"
      "v_rcp_f32 v228, v228\n"
      "s_add_u32 s78, s75, 0xa04000\n"
      "buffer_load_dwordx4 a[96:99], v174, s[56:59], s78 offen offset:0 sc1\n"
      "v_rcp_f32 v229, v229\n"
      "v_rcp_f32 v230, v230\n"
      "v_rcp_f32 v231, v231\n"
      "v_pk_mul_f32 v[224:225], v[224:225], v[136:137]\n"
      "v_pk_mul_f32 v[226:227], v[226:227], v[138:139]\n"
      "v_pk_mul_f32 v[228:229], v[228:229], v[140:141]\n"
      "v_pk_mul_f32 v[230:231], v[230:231], v[142:143]\n"
      "v_cvt_pk_bf16_f32 v236, v224, v225\n"
      "v_cvt_pk_bf16_f32 v237, v226, v227\n"
      "v_cvt_pk_bf16_f32 v238, v228, v229\n"
      "buffer_load_dwordx4 a[100:103], v174, s[56:59], s78 offen offset:1024 sc1\n"
      "v_cvt_pk_bf16_f32 v239, v230, v231\n"
      "buffer_store_dwordx4 v[236:239], v174, s[56:59], s23 offen offset:1024 sc1\n"
      "v_pk_mul_f32 v[224:225], v[144:145], v[144:145]\n"
      "v_pk_mul_f32 v[226:227], v[146:147], v[146:147]\n"
      "v_pk_mul_f32 v[228:229], v[148:149], v[148:149]\n"
      "v_pk_mul_f32 v[230:231], v[150:151], v[150:151]\n"
      "v_pk_fma_f32 v[224:225], v[224:225], v[28:29], v[28:29] op_sel:[0,1,0] op_sel_hi:[1,1,0]\n"
      "v_pk_fma_f32 v[226:227], v[226:227], v[28:29], v[28:29] op_sel:[0,1,0] op_sel_hi:[1,1,0]\n"
      "v_pk_fma_f32 v[228:229], v[228:229], v[28:29], v[28:29] op_sel:[0,1,0] op_sel_hi:[1,1,0]\n"
      "v_pk_fma_f32 v[230:231], v[230:231], v[28:29], v[28:29] op_sel:[0,1,0] op_sel_hi:[1,1,0]\n"
      "v_pk_mul_f32 v[224:225], v[224:225], v[144:145]\n"
      "v_pk_mul_f32 v[226:227], v[226:227], v[146:147]\n"
      "v_pk_mul_f32 v[228:229], v[228:229], v[148:149]\n"
      "v_pk_mul_f32 v[230:231], v[230:231], v[150:151]\n"
      "v_exp_f32 v224, v224\n"
      "buffer_load_dwordx4 a[104:107], v174, s[56:59], s78 offen offset:2048 sc1\n"
      "v_exp_f32 v225, v225\n"
      "v_exp_f32 v226, v226\n"
      "v_exp_f32 v227, v227\n"
      "v_exp_f32 v228, v228\n"
      "v_exp_f32 v229, v229\n"
      "v_exp_f32 v230, v230\n"
      "v_exp_f32 v231, v231\n"
      "buffer_load_dwordx4 a[108:111], v174, s[56:59], s78 offen offset:3072 sc1\n"
      "v_pk_fma_f32 v[224:225], v[224:225], v[30:31], v[30:31] op_sel_hi:[1,0,0]\n"
      "v_pk_fma_f32 v[226:227], v[226:227], v[30:31], v[30:31] op_sel_hi:[1,0,0]\n"
      "v_pk_fma_f32 v[228:229], v[228:229], v[30:31], v[30:31] op_sel_hi:[1,0,0]\n"
      "v_pk_fma_f32 v[230:231], v[230:231], v[30:31], v[30:31] op_sel_hi:[1,0,0]\n"
      "v_rcp_f32 v224, v224\n"
      "v_rcp_f32 v225, v225\n"
      "v_rcp_f32 v226, v226\n"
      "v_rcp_f32 v227, v227\n"
      "v_rcp_f32 v228, v228\n"
      "s_add_u32 s78, s75, 0xa06000\n"
      "buffer_load_dwordx4 a[112:115], v174, s[56:59], s78 offen offset:0 sc1\n"
      "v_rcp_f32 v229, v229\n"
      "v_rcp_f32 v230, v230\n"
      "v_rcp_f32 v231, v231\n"
      "v_pk_mul_f32 v[224:225], v[224:225], v[144:145]\n"
      "v_pk_mul_f32 v[226:227], v[226:227], v[146:147]\n"
      "v_pk_mul_f32 v[228:229], v[228:229], v[148:149]\n"
      "v_pk_mul_f32 v[230:231], v[230:231], v[150:151]\n"
      "v_cvt_pk_bf16_f32 v232, v224, v225\n"
      "v_cvt_pk_bf16_f32 v233, v226, v227\n"
      "v_cvt_pk_bf16_f32 v234, v228, v229\n"
      "buffer_load_dwordx4 a[116:119], v174, s[56:59], s78 offen offset:1024 sc1\n"
      "v_cvt_pk_bf16_f32 v235, v230, v231\n"
      "buffer_store_dwordx4 v[232:235], v174, s[56:59], s23 offen offset:2048 sc1\n"
      "v_pk_mul_f32 v[224:225], v[152:153], v[152:153]\n"
      "v_pk_mul_f32 v[226:227], v[154:155], v[154:155]\n"
      "v_pk_mul_f32 v[228:229], v[156:157], v[156:157]\n"
      "v_pk_mul_f32 v[230:231], v[158:159], v[158:159]\n"
      "v_pk_fma_f32 v[224:225], v[224:225], v[28:29], v[28:29] op_sel:[0,1,0] op_sel_hi:[1,1,0]\n"
      "v_pk_fma_f32 v[226:227], v[226:227], v[28:29], v[28:29] op_sel:[0,1,0] op_sel_hi:[1,1,0]\n"
      "v_pk_fma_f32 v[228:229], v[228:229], v[28:29], v[28:29] op_sel:[0,1,0] op_sel_hi:[1,1,0]\n"
      "v_pk_fma_f32 v[230:231], v[230:231], v[28:29], v[28:29] op_sel:[0,1,0] op_sel_hi:[1,1,0]\n"
      "v_pk_mul_f32 v[224:225], v[224:225], v[152:153]\n"
      "v_pk_mul_f32 v[226:227], v[226:227], v[154:155]\n"
      "v_pk_mul_f32 v[228:229], v[228:229], v[156:157]\n"
      "v_pk_mul_f32 v[230:231], v[230:231], v[158:159]\n"
      "v_exp_f32 v224, v224\n"
      "buffer_load_dwordx4 a[120:123], v174, s[56:59], s78 offen offset:2048 sc1\n"
      "v_exp_f32 v225, v225\n"
      "v_exp_f32 v226, v226\n"
      "v_exp_f32 v227, v227\n"
      "v_exp_f32 v228, v228\n"
      "v_exp_f32 v229, v229\n"
      "v_exp_f32 v230, v230\n"
      "v_exp_f32 v231, v231\n"
      "buffer_load_dwordx4 a[124:127], v174, s[56:59], s78 offen offset:3072 sc1\n"
      "v_pk_fma_f32 v[224:225], v[224:225], v[30:31], v[30:31] op_sel_hi:[1,0,0]\n"
      "v_pk_fma_f32 v[226:227], v[226:227], v[30:31], v[30:31] op_sel_hi:[1,0,0]\n"
      "v_pk_fma_f32 v[228:229], v[228:229], v[30:31], v[30:31] op_sel_hi:[1,0,0]\n"
      "v_pk_fma_f32 v[230:231], v[230:231], v[30:31], v[30:31] op_sel_hi:[1,0,0]\n"
      "v_rcp_f32 v224, v224\n"
      "v_rcp_f32 v225, v225\n"
      "v_rcp_f32 v226, v226\n"
      "v_rcp_f32 v227, v227\n"
      "v_rcp_f32 v228, v228\n"
      "buffer_load_dwordx4 v23, s[48:51], s17 offen offset:2048 lds\n"
      "v_rcp_f32 v229, v229\n"
      "v_rcp_f32 v230, v230\n"
      "v_rcp_f32 v231, v231\n"
      "v_pk_mul_f32 v[224:225], v[224:225], v[152:153]\n"
      "v_pk_mul_f32 v[226:227], v[226:227], v[154:155]\n"
      "v_pk_mul_f32 v[228:229], v[228:229], v[156:157]\n"
      "v_pk_mul_f32 v[230:231], v[230:231], v[158:159]\n"
      "v_cvt_pk_bf16_f32 v236, v224, v225\n"
      "v_cvt_pk_bf16_f32 v237, v226, v227\n"
      "v_cvt_pk_bf16_f32 v238, v228, v229\n"
      "buffer_load_dwordx4 v23, s[48:51], s18 offen offset:3072 lds\n"
      "v_cvt_pk_bf16_f32 v239, v230, v231\n"
      "buffer_store_dwordx4 v[236:239], v174, s[56:59], s23 offen offset:3072 sc1\n"
      "L_merge:\n"
      "s_mov_b64 exec, 0xff\n"
      "buffer_load_dwordx2 v[24:25], v201, s[56:59], s82 offen sc1\n"
      "s_mov_b64 exec, -1\n"
      "s_waitcnt vmcnt(22)\n"
      "v_mfma_f32_32x32x16_bf16 v[32:47], a[128:131], a[64:67], v[32:47]\n"
      "v_and_b32 v168, 31, v0\n"
      "v_lshl_add_u32 v168, s10, 5, v168\n"
      "v_lshlrev_b32 v168, 8, v168\n"
      "s_mul_i32 s22, s11, 0xfffe4080\n"
      "s_add_u32 s22, s22, 0x24000\n"
      "v_mfma_f32_32x32x16_bf16 v[48:63], a[132:135], a[64:67], v[48:63]\n"
      "s_add_u32 s78, s75, 0xa08000\n"
      "buffer_load_dwordx4 a[128:131], v174, s[56:59], s78 offen offset:0 sc1\n"
      "s_waitcnt vmcnt(22)\n"
      "v_mfma_f32_32x32x16_bf16 v[32:47], a[136:139], a[68:71], v[32:47]\n"
      "v_add_u32 v168, s22, v168\n"
      "v_lshrrev_b32 v169, 5, v0\n"
      "v_xor_b32 v169, v169, v0\n"
      "v_and_b32 v169, 7, v169\n"
      "v_mfma_f32_32x32x16_bf16 v[48:63], a[140:143], a[68:71], v[48:63]\n"
      "buffer_load_dwordx4 a[132:135], v174, s[56:59], s78 offen offset:1024 sc1\n"
      "s_waitcnt vmcnt(22)\n"
      "v_mfma_f32_32x32x16_bf16 v[32:47], a[144:147], a[72:75], v[32:47]\n"
      "v_xor_b32 v160, 0, v169\n"
      "v_lshl_add_u32 v160, v160, 4, v168\n"
      "v_xor_b32 v164, 0x80, v160\n"
      "v_mfma_f32_32x32x16_bf16 v[48:63], a[148:151], a[72:75], v[48:63]\n"
      "buffer_load_dwordx4 a[136:139], v174, s[56:59], s78 offen offset:2048 sc1\n"
      "s_waitcnt vmcnt(22)\n"
      "v_mfma_f32_32x32x16_bf16 v[32:47], a[152:155], a[76:79], v[32:47]\n"
      "v_xor_b32 v161, 2, v169\n"
      "v_lshl_add_u32 v161, v161, 4, v168\n"
      "v_xor_b32 v165, 0x80, v161\n"
      "v_mfma_f32_32x32x16_bf16 v[48:63], a[156:159], a[76:79], v[48:63]\n"
      "buffer_load_dwordx4 a[140:143], v174, s[56:59], s78 offen offset:3072 sc1\n"
      "s_waitcnt vmcnt(22)\n"
      "v_mfma_f32_32x32x16_bf16 v[32:47], a[160:163], a[80:83], v[32:47]\n"
      "v_xor_b32 v162, 4, v169\n"
      "v_lshl_add_u32 v162, v162, 4, v168\n"
      "v_xor_b32 v166, 0x80, v162\n"
      "v_mfma_f32_32x32x16_bf16 v[48:63], a[164:167], a[80:83], v[48:63]\n"
      "s_add_u32 s78, s75, 0xa0a000\n"
      "buffer_load_dwordx4 a[144:147], v174, s[56:59], s78 offen offset:0 sc1\n"
      "s_waitcnt vmcnt(22)\n"
      "v_mfma_f32_32x32x16_bf16 v[32:47], a[168:171], a[84:87], v[32:47]\n"
      "v_xor_b32 v163, 6, v169\n"
      "v_lshl_add_u32 v163, v163, 4, v168\n"
      "v_xor_b32 v167, 0x80, v163\n"
      "v_mfma_f32_32x32x16_bf16 v[48:63], a[172:175], a[84:87], v[48:63]\n"
      "buffer_load_dwordx4 a[148:151], v174, s[56:59], s78 offen offset:1024 sc1\n"
      "s_waitcnt vmcnt(21)\n"
      "v_mfma_f32_32x32x16_bf16 v[32:47], a[176:179], a[88:91], v[32:47]\n"
      "v_lshrrev_b32 v168, 3, v0\n"
      "v_and_b32 v169, 7, v0\n"
      "v_lshlrev_b32 v169, 1, v169\n"
      "v_mfma_f32_32x32x16_bf16 v[48:63], a[180:183], a[88:91], v[48:63]\n"
      "buffer_load_dwordx4 a[152:155], v174, s[56:59], s78 offen offset:2048 sc1\n"
      "s_waitcnt vmcnt(21)\n"
      "v_mfma_f32_32x32x16_bf16 v[32:47], a[184:187], a[92:95], v[32:47]\n"
      "s_waitcnt vmcnt(8)\n"
      "s_mov_b64 exec, 1\n"
      "s_xor_b32 s86, s63, 0xc0000\n"
      "buffer_store_dwordx2 v[26:27], v1, s[56:59], s86 offen sc1\n"
      "s_mov_b64 exec, -1\n"
      "v_mfma_f32_32x32x16_bf16 v[48:63], a[188:191], a[92:95], v[48:63]\n"
      "v_xor_b32 v169, v169, v168\n"
      "v_lshlrev_b32 v169, 4, v169\n"
      "v_lshl_add_u32 v168, v168, 8, v169\n"
      "s_add_u32 s22, s15, 0x24000\n"
      "v_add_u32 v168, s22, v168\n"
      "v_mfma_f32_32x32x16_bf16 v[32:47], a[0:3], a[96:99], v[32:47]\n"
      "buffer_load_dwordx4 a[156:159], v174, s[56:59], s78 offen offset:3072 sc1\n"
      "v_mfma_f32_32x32x16_bf16 v[48:63], a[4:7], a[96:99], v[48:63]\n"
      "v_xor_b32 v169, 16, v168\n"
      "v_add_u32 v170, 0xfffe4000, v168\n"
      "v_add_u32 v171, 0xfffe4000, v169\n"
      "v_mfma_f32_32x32x16_bf16 v[32:47], a[8:11], a[100:103], v[32:47]\n"
      "s_add_u32 s78, s75, 0xa0c000\n"
      "buffer_load_dwordx4 a[160:163], v174, s[56:59], s78 offen offset:0 sc1\n"
      "v_mfma_f32_32x32x16_bf16 v[48:63], a[12:15], a[100:103], v[48:63]\n"
      "v_lshlrev_b32 v224, 16, v80\n"
      "v_and_b32 v225, s65, v80\n"
      "v_mfma_f32_32x32x16_bf16 v[32:47], a[16:19], a[104:107], v[32:47]\n"
      "buffer_load_dwordx4 a[164:167], v174, s[56:59], s78 offen offset:1024 sc1\n"
      "v_mfma_f32_32x32x16_bf16 v[48:63], a[20:23], a[104:107], v[48:63]\n"
      "v_lshlrev_b32 v226, 16, v81\n"
      "v_and_b32 v227, s65, v81\n"
      "v_lshlrev_b32 v228, 16, v82\n"
      "v_mfma_f32_32x32x16_bf16 v[32:47], a[24:27], a[108:111], v[32:47]\n"
      "buffer_load_dwordx4 a[168:171], v174, s[56:59], s78 offen offset:2048 sc1\n"
      "v_mfma_f32_32x32x16_bf16 v[48:63], a[28:31], a[108:111], v[48:63]\n"
      "v_and_b32 v229, s65, v82\n"
      "v_lshlrev_b32 v230, 16, v83\n"
      "v_and_b32 v231, s65, v83\n"
      "v_mfma_f32_32x32x16_bf16 v[32:47], a[32:35], a[112:115], v[32:47]\n"
      "buffer_load_dwordx4 a[172:175], v174, s[56:59], s78 offen offset:3072 sc1\n"
      "v_mfma_f32_32x32x16_bf16 v[48:63], a[36:39], a[112:115], v[48:63]\n"
      "v_lshlrev_b32 v232, 16, v84\n"
      "v_and_b32 v233, s65, v84\n"
      "v_lshlrev_b32 v234, 16, v85\n"
      "v_mfma_f32_32x32x16_bf16 v[32:47], a[40:43], a[116:119], v[32:47]\n"
      "v_and_b32 v235, s65, v85\n"
      "v_lshlrev_b32 v236, 16, v86\n"
      "v_mfma_f32_32x32x16_bf16 v[48:63], a[44:47], a[116:119], v[48:63]\n"
      "v_and_b32 v237, s65, v86\n"
      "s_mov_b64 exec, 0xff\n"
      "s_waitcnt vmcnt(13)\n"
      "v_cmp_eq_u64 vcc, s[60:61], v[24:25]\n"
      "s_nop 0\n"
      "s_cmp_eq_u64 vcc, exec\n"
      "s_cbranch_scc0 L_slow_o\n"
      "L_ok_o:\n"
      "s_mov_b64 exec, -1\n"
      "v_mfma_f32_32x32x16_bf16 v[32:47], a[48:51], a[120:123], v[32:47]\n"
      "s_add_u32 s78, s73, 0x800000\n"
      "buffer_load_dwordx4 a[0:3], v174, s[56:59], s78 offen offset:0 sc1\n"
      "v_mfma_f32_32x32x16_bf16 v[48:63], a[52:55], a[120:123], v[48:63]\n"
      "s_barrier\n"
      "ds_read_b64_tr_b16 a[192:193], v10 offset:0\n"
      "ds_read_b64_tr_b16 a[194:195], v10 offset:4096\n"
      "ds_read_b64_tr_b16 a[196:197], v13 offset:0\n"
      "ds_read_b64_tr_b16 a[198:199], v13 offset:4096\n"
      "v_mfma_f32_32x32x16_bf16 v[32:47], a[56:59], a[124:127], v[32:47]\n"
      "buffer_load_dwordx4 a[4:7], v174, s[56:59], s78 offen offset:1024 sc1\n"
      "v_mfma_f32_32x32x16_bf16 v[48:63], a[60:63], a[124:127], v[48:63]\n"
      "ds_read_b64_tr_b16 a[200:201], v10 offset:1024\n"
      "ds_read_b64_tr_b16 a[202:203], v10 offset:5120\n"
      "ds_read_b64_tr_b16 a[204:205], v13 offset:1024\n"
      "ds_read_b64_tr_b16 a[206:207], v13 offset:5120\n"
      "v_lshlrev_b32 v238, 16, v87\n"
      "v_and_b32 v239, s65, v87\n"
      "s_waitcnt vmcnt(14) lgkmcnt(6)\n"
      "v_mfma_f32_32x32x16_bf16 v[32:47], a[192:195], a[128:131], v[32:47]\n"
      "buffer_load_dwordx4 a[8:11], v174, s[56:59], s78 offen offset:2048 sc1\n"
      "s_waitcnt lgkmcnt(4)\n"
      "v_mfma_f32_32x32x16_bf16 v[48:63], a[196:199], a[128:131], v[48:63]\n"
      "ds_read_b64_tr_b16 a[208:209], v10 offset:2048\n"
      "ds_read_b64_tr_b16 a[210:211], v10 offset:6144\n"
      "ds_read_b64_tr_b16 a[212:213], v13 offset:2048\n"
      "ds_read_b64_tr_b16 a[214:215], v13 offset:6144\n"
      "s_waitcnt vmcnt(14) lgkmcnt(6)\n"
      "v_mfma_f32_32x32x16_bf16 v[32:47], a[200:203], a[132:135], v[32:47]\n"
      "buffer_load_dwordx4 a[12:15], v174, s[56:59], s78 offen offset:3072 sc1\n"
      "s_waitcnt lgkmcnt(4)\n"
      "v_mfma_f32_32x32x16_bf16 v[48:63], a[204:207], a[132:135], v[48:63]\n"
      "ds_read_b64_tr_b16 a[216:217], v10 offset:3072\n"
      "ds_read_b64_tr_b16 a[218:219], v10 offset:7168\n"
      "ds_read_b64_tr_b16 a[220:221], v13 offset:3072\n"
      "ds_read_b64_tr_b16 a[222:223], v13 offset:7168\n"
      "s_waitcnt vmcnt(14) lgkmcnt(6)\n"
      "v_mfma_f32_32x32x16_bf16 v[32:47], a[208:211], a[136:139], v[32:47]\n"
      "s_add_u32 s78, s73, 0x801000\n"
      "buffer_load_dwordx4 a[16:19], v174, s[56:59], s78 offen offset:0 sc1\n"
      "s_waitcnt lgkmcnt(4)\n"
      "v_mfma_f32_32x32x16_bf16 v[48:63], a[212:215], a[136:139], v[48:63]\n"
      "ds_read_b64_tr_b16 a[192:193], v10 offset:16384\n"
      "ds_read_b64_tr_b16 a[194:195], v10 offset:20480\n"
      "ds_read_b64_tr_b16 a[196:197], v13 offset:16384\n"
      "ds_read_b64_tr_b16 a[198:199], v13 offset:20480\n"
      "s_waitcnt vmcnt(14) lgkmcnt(6)\n"
      "v_mfma_f32_32x32x16_bf16 v[32:47], a[216:219], a[140:143], v[32:47]\n"
      "buffer_load_dwordx4 a[20:23], v174, s[56:59], s78 offen offset:1024 sc1\n"
      "s_waitcnt lgkmcnt(4)\n"
      "v_mfma_f32_32x32x16_bf16 v[48:63], a[220:223], a[140:143], v[48:63]\n"
      "ds_read_b64_tr_b16 a[200:201], v10 offset:17408\n"
      "ds_read_b64_tr_b16 a[202:203], v10 offset:21504\n"
      "ds_read_b64_tr_b16 a[204:205], v13 offset:17408\n"
      "ds_read_b64_tr_b16 a[206:207], v13 offset:21504\n"
      "s_waitcnt vmcnt(14) lgkmcnt(6)\n"
      "v_mfma_f32_32x32x16_bf16 v[32:47], a[192:195], a[144:147], v[32:47]\n"
      "buffer_load_dwordx4 a[24:27], v174, s[56:59], s78 offen offset:2048 sc1\n"
      "s_waitcnt lgkmcnt(4)\n"
      "v_mfma_f32_32x32x16_bf16 v[48:63], a[196:199], a[144:147], v[48:63]\n"
      "ds_read_b64_tr_b16 a[208:209], v10 offset:18432\n"
      "ds_read_b64_tr_b16 a[210:211], v10 offset:22528\n"
      "ds_read_b64_tr_b16 a[212:213], v13 offset:18432\n"
      "ds_read_b64_tr_b16 a[214:215], v13 offset:22528\n"
      "s_waitcnt vmcnt(14) lgkmcnt(6)\n"
      "v_mfma_f32_32x32x16_bf16 v[32:47], a[200:203], a[148:151], v[32:47]\n"
      "buffer_load_dwordx4 a[28:31], v174, s[56:59], s78 offen offset:3072 sc1\n"
      "s_waitcnt lgkmcnt(4)\n"
      "v_mfma_f32_32x32x16_bf16 v[48:63], a[204:207], a[148:151], v[48:63]\n"
      "ds_read_b64_tr_b16 a[216:217], v10 offset:19456\n"
      "ds_read_b64_tr_b16 a[218:219], v10 offset:23552\n"
      "ds_read_b64_tr_b16 a[220:221], v13 offset:19456\n"
      "ds_read_b64_tr_b16 a[222:223], v13 offset:23552\n"
      "s_waitcnt vmcnt(14) lgkmcnt(6)\n"
      "v_mfma_f32_32x32x16_bf16 v[32:47], a[208:211], a[152:155], v[32:47]\n"
      "s_add_u32 s78, s73, 0x802000\n"
      "buffer_load_dwordx4 a[32:35], v174, s[56:59], s78 offen offset:0 sc1\n"
      "s_waitcnt lgkmcnt(4)\n"
      "v_mfma_f32_32x32x16_bf16 v[48:63], a[212:215], a[152:155], v[48:63]\n"
      "s_waitcnt lgkmcnt(0)\n"
      "s_barrier\n"
      "ds_read_b64_tr_b16 a[192:193], v10 offset:49152\n"
      "ds_read_b64_tr_b16 a[194:195], v10 offset:53248\n"
      "ds_read_b64_tr_b16 a[196:197], v13 offset:49152\n"
      "ds_read_b64_tr_b16 a[198:199], v13 offset:53248\n"
      "buffer_load_dwordx4 a[36:39], v174, s[56:59], s78 offen offset:1024 sc1\n"
      "s_waitcnt vmcnt(14)\n"
      "v_mfma_f32_32x32x16_bf16 v[32:47], a[216:219], a[156:159], v[32:47]\n"
      "ds_read_b64_tr_b16 a[200:201], v10 offset:50176\n"
      "ds_read_b64_tr_b16 a[202:203], v10 offset:54272\n"
      "ds_read_b64_tr_b16 a[204:205], v13 offset:50176\n"
      "ds_read_b64_tr_b16 a[206:207], v13 offset:54272\n"
      "v_mfma_f32_32x32x16_bf16 v[48:63], a[220:223], a[156:159], v[48:63]\n"
      "buffer_load_dwordx4 a[40:43], v174, s[56:59], s78 offen offset:2048 sc1\n"
      "s_waitcnt vmcnt(14) lgkmcnt(6)\n"
      "v_mfma_f32_32x32x16_bf16 v[32:47], a[192:195], a[160:163], v[32:47]\n"
      "ds_read_b64_tr_b16 a[208:209], v10 offset:51200\n"
      "ds_read_b64_tr_b16 a[210:211], v10 offset:55296\n"
      "ds_read_b64_tr_b16 a[212:213], v13 offset:51200\n"
      "ds_read_b64_tr_b16 a[214:215], v13 offset:55296\n"
      "s_waitcnt lgkmcnt(8)\n"
      "v_mfma_f32_32x32x16_bf16 v[48:63], a[196:199], a[160:163], v[48:63]\n"
      "buffer_load_dwordx4 a[44:47], v174, s[56:59], s78 offen offset:3072 sc1\n"
      "s_waitcnt vmcnt(14) lgkmcnt(4)\n"
      "v_mfma_f32_32x32x16_bf16 v[32:47], a[200:203], a[164:167], v[32:47]\n"
      "ds_read_b64_tr_b16 a[216:217], v10 offset:52224\n"
      "ds_read_b64_tr_b16 a[218:219], v10 offset:56320\n"
      "ds_read_b64_tr_b16 a[220:221], v13 offset:52224\n"
      "ds_read_b64_tr_b16 a[222:223], v13 offset:56320\n"
      "v_mfma_f32_32x32x16_bf16 v[48:63], a[204:207], a[164:167], v[48:63]\n"
      "s_add_u32 s78, s73, 0x803000\n"
      "buffer_load_dwordx4 a[48:51], v174, s[56:59], s78 offen offset:0 sc1\n"
      "s_waitcnt vmcnt(14) lgkmcnt(4)\n"
      "v_mfma_f32_32x32x16_bf16 v[32:47], a[208:211], a[168:171], v[32:47]\n"
      "ds_read_b64_tr_b16 a[192:193], v9 offset:16384\n"
      "ds_read_b64_tr_b16 a[194:195], v9 offset:20480\n"
      "ds_read_b64_tr_b16 a[196:197], v12 offset:16384\n"
      "ds_read_b64_tr_b16 a[198:199], v12 offset:20480\n"
      "v_mfma_f32_32x32x16_bf16 v[48:63], a[212:215], a[168:171], v[48:63]\n"
      "buffer_load_dwordx4 a[52:55], v174, s[56:59], s78 offen offset:1024 sc1\n"
      "s_waitcnt vmcnt(14) lgkmcnt(4)\n"
      "v_mfma_f32_32x32x16_bf16 v[32:47], a[216:219], a[172:175], v[32:47]\n"
      "ds_read_b64_tr_b16 a[200:201], v9 offset:17408\n"
      "ds_read_b64_tr_b16 a[202:203], v9 offset:21504\n"
      "ds_read_b64_tr_b16 a[204:205], v12 offset:17408\n"
      "ds_read_b64_tr_b16 a[206:207], v12 offset:21504\n"
      "v_mfma_f32_32x32x16_bf16 v[48:63], a[220:223], a[172:175], v[48:63]\n"
      "buffer_load_dwordx4 a[56:59], v174, s[56:59], s78 offen offset:2048 sc1\n"
      "s_mov_b64 exec, 0x7f\n"
      "buffer_load_dwordx2 v[24:25], v201, s[56:59], s84 offen sc1\n"
      "s_mov_b64 exec, -1\n"
      "s_waitcnt vmcnt(15) lgkmcnt(0)\n"
      "v_mfma_f32_32x32x16_bf16 v[96:111], a[192:195], a[0:3], 0\n"
      "ds_read_b64_tr_b16 a[208:209], v9 offset:18432\n"
      "ds_read_b64_tr_b16 a[210:211], v9 offset:22528\n"
      "ds_read_b64_tr_b16 a[212:213], v12 offset:18432\n"
      "ds_read_b64_tr_b16 a[214:215], v12 offset:22528\n"
      "v_mfma_f32_32x32x16_bf16 v[112:127], a[196:199], a[0:3], 0\n"
      "buffer_load_dwordx4 a[60:63], v174, s[56:59], s78 offen offset:3072 sc1\n"
      "s_waitcnt vmcnt(15)\n"
      "v_mfma_f32_32x32x16_bf16 v[96:111], a[200:203], a[4:7], v[96:111]\n"
      "ds_read_b64_tr_b16 a[216:217], v9 offset:19456\n"
      "ds_read_b64_tr_b16 a[218:219], v9 offset:23552\n"
      "ds_read_b64_tr_b16 a[220:221], v12 offset:19456\n"
      "ds_read_b64_tr_b16 a[222:223], v12 offset:23552\n"
      "v_mfma_f32_32x32x16_bf16 v[112:127], a[204:207], a[4:7], v[112:127]\n"
      "s_add_u32 m0, s15, 0x10000\n"
      "v_add_u32 v23, 0x20000, v23\n"
      "buffer_load_dwordx4 v23, s[48:51], 0 offen offset:0 lds\n"
      "s_waitcnt vmcnt(15) lgkmcnt(4)\n"
      "v_mfma_f32_32x32x16_bf16 v[96:111], a[208:211], a[8:11], v[96:111]\n"
      "v_mfma_f32_32x32x16_bf16 v[112:127], a[212:215], a[8:11], v[112:127]\n"
      "s_waitcnt lgkmcnt(0)\n"
      "s_barrier\n"
      "ds_read_b64_tr_b16 a[192:193], v9 offset:0\n"
      "ds_read_b64_tr_b16 a[194:195], v9 offset:4096\n"
      "buffer_load_dwordx4 v23, s[48:51], s16 offen offset:1024 lds\n"
      "s_waitcnt vmcnt(15)\n"
      "v_mfma_f32_32x32x16_bf16 v[96:111], a[216:219], a[12:15], v[96:111]\n"
      "ds_read_b64_tr_b16 a[196:197], v12 offset:0\n"
      "ds_read_b64_tr_b16 a[198:199], v12 offset:4096\n"
      "ds_read_b64_tr_b16 a[200:201], v9 offset:1024\n"
      "ds_read_b64_tr_b16 a[202:203], v9 offset:5120\n"
      "v_mfma_f32_32x32x16_bf16 v[112:127], a[220:223], a[12:15], v[112:127]\n"
      "buffer_load_dwordx4 v23, s[48:51], s17 offen offset:2048 lds\n"
      "s_waitcnt vmcnt(15) lgkmcnt(4)\n"
      "v_mfma_f32_32x32x16_bf16 v[96:111], a[192:195], a[16:19], v[96:111]\n"
      "ds_read_b64_tr_b16 a[204:205], v12 offset:1024\n"
      "ds_read_b64_tr_b16 a[206:207], v12 offset:5120\n"
      "ds_read_b64_tr_b16 a[208:209], v9 offset:2048\n"
      "ds_read_b64_tr_b16 a[210:211], v9 offset:6144\n"
      "s_waitcnt lgkmcnt(6)\n"
      "v_mfma_f32_32x32x16_bf16 v[112:127], a[196:199], a[16:19], v[112:127]\n"
      "buffer_load_dwordx4 v23, s[48:51], s18 offen offset:3072 lds\n"
      "s_add_u32 m0, s15, 0x14000\n"
      "v_add_u32 v23, 0x20000, v23\n"
      "buffer_load_dwordx4 v23, s[48:51], 0 offen offset:0 lds\n"
      "buffer_load_dwordx4 v23, s[48:51], s16 offen offset:1024 lds\n"
      "buffer_load_dwordx4 v23, s[48:51], s17 offen offset:2048 lds\n"
      "buffer_load_dwordx4 v23, s[48:51], s18 offen offset:3072 lds\n"
      "s_add_u32 m0, s15, 0x1c000\n"
      "v_add_u32 v23, 0x20000, v23\n"
      "buffer_load_dwordx4 v23, s[48:51], 0 offen offset:0 lds\n"
      "buffer_load_dwordx4 v23, s[48:51], s16 offen offset:1024 lds\n"
      "s_mov_b64 exec, 0x7f\n"
      "s_waitcnt vmcnt(11)\n"
      "v_cmp_eq_u64 vcc, s[60:61], v[24:25]\n"
      "s_nop 0\n"
      "s_cmp_eq_u64 vcc, exec\n"
      "s_cbranch_scc0 L_slow_b\n"
      "L_ok_b:\n"
      "s_mov_b64 exec, -1\n"
      "s_waitcnt lgkmcnt(0)\n"
      "v_mfma_f32_32x32x16_bf16 v[96:111], a[200:203], a[20:23], v[96:111]\n"
      "s_add_u32 s78, s75, 0xa01000\n"
      "buffer_load_dwordx4 a[176:179], v174, s[56:59], s78 offen offset:0 sc1\n"
      "ds_read_b64_tr_b16 a[212:213], v12 offset:2048\n"
      "ds_read_b64_tr_b16 a[214:215], v12 offset:6144\n"
      "v_mfma_f32_32x32x16_bf16 v[112:127], a[204:207], a[20:23], v[112:127]\n"
      "ds_read_b64_tr_b16 a[216:217], v9 offset:3072\n"
      "ds_read_b64_tr_b16 a[218:219], v9 offset:7168\n"
      "ds_read_b64_tr_b16 a[220:221], v12 offset:3072\n"
      "ds_read_b64_tr_b16 a[222:223], v12 offset:7168\n"
      "v_mfma_f32_32x32x16_bf16 v[96:111], a[208:211], a[24:27], v[96:111]\n"
      "buffer_load_dwordx4 a[180:183], v174, s[56:59], s78 offen offset:1024 sc1\n"
      "s_waitcnt lgkmcnt(4)\n"
      "v_mfma_f32_32x32x16_bf16 v[112:127], a[212:215], a[24:27], v[112:127]\n"
      "ds_read_b64_tr_b16 a[192:193], v9 offset:32768\n"
      "ds_read_b64_tr_b16 a[194:195], v9 offset:36864\n"
      "ds_read_b64_tr_b16 a[196:197], v12 offset:32768\n"
      "ds_read_b64_tr_b16 a[198:199], v12 offset:36864\n"
      "s_waitcnt lgkmcnt(6)\n"
      "v_mfma_f32_32x32x16_bf16 v[96:111], a[216:219], a[28:31], v[96:111]\n"
      "buffer_load_dwordx4 a[184:187], v174, s[56:59], s78 offen offset:2048 sc1\n"
      "s_waitcnt lgkmcnt(4)\n"
      "v_mfma_f32_32x32x16_bf16 v[112:127], a[220:223], a[28:31], v[112:127]\n"
      "ds_read_b64_tr_b16 a[200:201], v9 offset:33792\n"
      "ds_read_b64_tr_b16 a[202:203], v9 offset:37888\n"
      "ds_read_b64_tr_b16 a[204:205], v12 offset:33792\n"
      "ds_read_b64_tr_b16 a[206:207], v12 offset:37888\n"
      "s_waitcnt lgkmcnt(6)\n"
      "v_mfma_f32_32x32x16_bf16 v[96:111], a[192:195], a[32:35], v[96:111]\n"
      "buffer_load_dwordx4 a[188:191], v174, s[56:59], s78 offen offset:3072 sc1\n"
      "s_waitcnt lgkmcnt(4)\n"
      "v_mfma_f32_32x32x16_bf16 v[112:127], a[196:199], a[32:35], v[112:127]\n"
      "ds_read_b64_tr_b16 a[208:209], v9 offset:34816\n"
      "ds_read_b64_tr_b16 a[210:211], v9 offset:38912\n"
      "ds_read_b64_tr_b16 a[212:213], v12 offset:34816\n"
      "ds_read_b64_tr_b16 a[214:215], v12 offset:38912\n"
      "s_waitcnt lgkmcnt(6)\n"
      "v_mfma_f32_32x32x16_bf16 v[96:111], a[200:203], a[36:39], v[96:111]\n"
      "s_add_u32 s78, s75, 0xa03000\n"
      "buffer_load_dwordx4 a[64:67], v174, s[56:59], s78 offen offset:0 sc1\n"
      "s_waitcnt lgkmcnt(4)\n"
      "v_mfma_f32_32x32x16_bf16 v[112:127], a[204:207], a[36:39], v[112:127]\n"
      "ds_read_b64_tr_b16 a[216:217], v9 offset:35840\n"
      "ds_read_b64_tr_b16 a[218:219], v9 offset:39936\n"
      "ds_read_b64_tr_b16 a[220:221], v12 offset:35840\n"
      "ds_read_b64_tr_b16 a[222:223], v12 offset:39936\n"
      "s_waitcnt lgkmcnt(6)\n"
      "v_mfma_f32_32x32x16_bf16 v[96:111], a[208:211], a[40:43], v[96:111]\n"
      "buffer_load_dwordx4 a[68:71], v174, s[56:59], s78 offen offset:1024 sc1\n"
      "s_waitcnt lgkmcnt(4)\n"
      "v_mfma_f32_32x32x16_bf16 v[112:127], a[212:215], a[40:43], v[112:127]\n"
      "s_waitcnt lgkmcnt(0)\n"
      "s_barrier\n"
      "ds_read_b64_tr_b16 a[192:193], v9 offset:49152\n"
      "ds_read_b64_tr_b16 a[194:195], v9 offset:53248\n"
      "ds_read_b64_tr_b16 a[196:197], v12 offset:49152\n"
      "ds_read_b64_tr_b16 a[198:199], v12 offset:53248\n"
      "buffer_load_dwordx4 a[72:75], v174, s[56:59], s78 offen offset:2048 sc1\n"
      "v_mfma_f32_32x32x16_bf16 v[96:111], a[216:219], a[44:47], v[96:111]\n"
      "ds_read_b64_tr_b16 a[200:201], v9 offset:50176\n"
      "ds_read_b64_tr_b16 a[202:203], v9 offset:54272\n"
      "ds_read_b64_tr_b16 a[204:205], v12 offset:50176\n"
      "ds_read_b64_tr_b16 a[206:207], v12 offset:54272\n"
      "v_mfma_f32_32x32x16_bf16 v[112:127], a[220:223], a[44:47], v[112:127]\n"
      "buffer_load_dwordx4 a[76:79], v174, s[56:59], s78 offen offset:3072 sc1\n"
      "s_waitcnt lgkmcnt(6)\n"
      "v_mfma_f32_32x32x16_bf16 v[96:111], a[192:195], a[48:51], v[96:111]\n"
      "ds_read_b64_tr_b16 a[208:209], v9 offset:51200\n"
      "ds_read_b64_tr_b16 a[210:211], v9 offset:55296\n"
      "ds_read_b64_tr_b16 a[212:213], v12 offset:51200\n"
      "ds_read_b64_tr_b16 a[214:215], v12 offset:55296\n"
      "s_waitcnt lgkmcnt(8)\n"
      "v_mfma_f32_32x32x16_bf16 v[112:127], a[196:199], a[48:51], v[112:127]\n"
      "s_add_u32 s78, s75, 0xa05000\n"
      "buffer_load_dwordx4 a[80:83], v174, s[56:59], s78 offen offset:0 sc1\n"
      "s_waitcnt lgkmcnt(4)\n"
      "v_mfma_f32_32x32x16_bf16 v[96:111], a[200:203], a[52:55], v[96:111]\n"
      "ds_read_b64_tr_b16 a[216:217], v9 offset:52224\n"
      "ds_read_b64_tr_b16 a[218:219], v9 offset:56320\n"
      "ds_read_b64_tr_b16 a[220:221], v12 offset:52224\n"
      "ds_read_b64_tr_b16 a[222:223], v12 offset:56320\n"
      "v_mfma_f32_32x32x16_bf16 v[112:127], a[204:207], a[52:55], v[112:127]\n"
      "buffer_load_dwordx4 a[84:87], v174, s[56:59], s78 offen offset:1024 sc1\n"
      "s_waitcnt lgkmcnt(4)\n"
      "v_mfma_f32_32x32x16_bf16 v[96:111], a[208:211], a[56:59], v[96:111]\n"
      "ds_read_b64_tr_b16 a[192:193], v11 offset:0\n"
      "ds_read_b64_tr_b16 a[194:195], v11 offset:4096\n"
      "ds_read_b64_tr_b16 a[196:197], v14 offset:0\n"
      "ds_read_b64_tr_b16 a[198:199], v14 offset:4096\n"
      "v_mfma_f32_32x32x16_bf16 v[112:127], a[212:215], a[56:59], v[112:127]\n"
      "buffer_load_dwordx4 a[88:91], v174, s[56:59], s78 offen offset:2048 sc1\n"
      "s_waitcnt vmcnt(21) lgkmcnt(4)\n"
      "v_mfma_f32_32x32x16_bf16 v[96:111], a[216:219], a[60:63], v[96:111]\n"
      "ds_read_b64_tr_b16 a[200:201], v11 offset:1024\n"
      "ds_read_b64_tr_b16 a[202:203], v11 offset:5120\n"
      "ds_read_b64_tr_b16 a[204:205], v14 offset:1024\n"
      "ds_read_b64_tr_b16 a[206:207], v14 offset:5120\n"
      "v_mfma_f32_32x32x16_bf16 v[112:127], a[220:223], a[60:63], v[112:127]\n"
      "buffer_load_dwordx4 v23, s[48:51], s17 offen offset:2048 lds\n"
      "s_waitcnt lgkmcnt(4)\n"
      "buffer_load_dwordx4 v23, s[48:51], s18 offen offset:3072 lds\n"
      "s_waitcnt vmcnt(12)\n"
      "v_mfma_f32_32x32x16_bf16 v[32:47], a[192:195], a[176:179], v[32:47]\n"
      "ds_read_b64_tr_b16 a[208:209], v11 offset:2048\n"
      "ds_read_b64_tr_b16 a[210:211], v11 offset:6144\n"
      "ds_read_b64_tr_b16 a[212:213], v14 offset:2048\n"
      "ds_read_b64_tr_b16 a[214:215], v14 offset:6144\n"
      "v_mfma_f32_32x32x16_bf16 v[48:63], a[196:199], a[176:179], v[48:63]\n"
      "buffer_load_dwordx4 a[92:95], v174, s[56:59], s78 offen offset:3072 sc1\n"
      "s_waitcnt vmcnt(12) lgkmcnt(4)\n"
      "v_mfma_f32_32x32x16_bf16 v[32:47], a[200:203], a[180:183], v[32:47]\n"
      "ds_read_b64_tr_b16 a[216:217], v11 offset:3072\n"
      "ds_read_b64_tr_b16 a[218:219], v11 offset:7168\n"
      "ds_read_b64_tr_b16 a[220:221], v14 offset:3072\n"
      "ds_read_b64_tr_b16 a[222:223], v14 offset:7168\n"
      "ds_write_b128 v160, v[96:99]\n"
      "s_add_u32 s78, s75, 0xa07000\n"
      "buffer_load_dwordx4 a[96:99], v174, s[56:59], s78 offen offset:0 sc1\n"
      "v_mfma_f32_32x32x16_bf16 v[48:63], a[204:207], a[180:183], v[48:63]\n"
      "ds_write_b128 v161, v[100:103]\n"
      "s_waitcnt vmcnt(12) lgkmcnt(4)\n"
      "v_mfma_f32_32x32x16_bf16 v[32:47], a[208:211], a[184:187], v[32:47]\n"
      "buffer_load_dwordx4 a[100:103], v174, s[56:59], s78 offen offset:1024 sc1\n"
      "v_mfma_f32_32x32x16_bf16 v[48:63], a[212:215], a[184:187], v[48:63]\n"
      "s_waitcnt lgkmcnt(0)\n"
      "s_barrier\n"
      "ds_read_b64_tr_b16 a[192:193], v10 offset:32768\n"
      "ds_read_b64_tr_b16 a[194:195], v10 offset:36864\n"
      "ds_read_b64_tr_b16 a[196:197], v13 offset:32768\n"
      "ds_read_b64_tr_b16 a[198:199], v13 offset:36864\n"
      "buffer_load_dwordx4 a[104:107], v174, s[56:59], s78 offen offset:2048 sc1\n"
      "s_waitcnt vmcnt(13)\n"
      "v_mfma_f32_32x32x16_bf16 v[32:47], a[216:219], a[188:191], v[32:47]\n"
      "ds_read_b64_tr_b16 a[200:201], v10 offset:33792\n"
      "ds_read_b64_tr_b16 a[202:203], v10 offset:37888\n"
      "ds_read_b64_tr_b16 a[204:205], v13 offset:33792\n"
      "ds_read_b64_tr_b16 a[206:207], v13 offset:37888\n"
      "ds_write_b128 v162, v[104:107]\n"
      "v_mfma_f32_32x32x16_bf16 v[48:63], a[220:223], a[188:191], v[48:63]\n"
      "buffer_load_dwordx4 a[108:111], v174, s[56:59], s78 offen offset:3072 sc1\n"
      "s_waitcnt vmcnt(13) lgkmcnt(5)\n"
      "v_mfma_f32_32x32x16_bf16 v[32:47], a[192:195], a[64:67], v[32:47]\n"
      "ds_read_b64_tr_b16 a[208:209], v10 offset:34816\n"
      "ds_read_b64_tr_b16 a[210:211], v10 offset:38912\n"
      "ds_read_b64_tr_b16 a[212:213], v13 offset:34816\n"
      "ds_read_b64_tr_b16 a[214:215], v13 offset:38912\n"
      "ds_write_b128 v163, v[108:111]\n"
      "s_add_u32 s78, s75, 0xa09000\n"
      "buffer_load_dwordx4 a[112:115], v174, s[56:59], s78 offen offset:0 sc1\n"
      "v_mfma_f32_32x32x16_bf16 v[48:63], a[196:199], a[64:67], v[48:63]\n"
      "ds_read_b64_tr_b16 a[216:217], v10 offset:35840\n"
      "ds_read_b64_tr_b16 a[218:219], v10 offset:39936\n"
      "ds_write_b128 v164, v[112:115]\n"
      "s_waitcnt vmcnt(13) lgkmcnt(4)\n"
      "v_mfma_f32_32x32x16_bf16 v[32:47], a[200:203], a[68:71], v[32:47]\n"
      "buffer_load_dwordx4 a[116:119], v174, s[56:59], s78 offen offset:1024 sc1\n"
      "v_mfma_f32_32x32x16_bf16 v[48:63], a[204:207], a[68:71], v[48:63]\n"
      "ds_read_b64_tr_b16 a[220:221], v13 offset:35840\n"
      "ds_read_b64_tr_b16 a[222:223], v13 offset:39936\n"
      "ds_read_b64_tr_b16 a[192:193], v10 offset:0\n"
      "ds_read_b64_tr_b16 a[194:195], v10 offset:4096\n"
      "ds_write_b128 v165, v[116:119]\n"
      "buffer_load_dwordx4 a[120:123], v174, s[56:59], s78 offen offset:2048 sc1\n"
      "s_waitcnt vmcnt(14)\n"
      "v_mfma_f32_32x32x16_bf16 v[32:47], a[208:211], a[72:75], v[32:47]\n"
      "ds_read_b64_tr_b16 a[196:197], v13 offset:0\n"
      "ds_read_b64_tr_b16 a[198:199], v13 offset:4096\n"
      "ds_write_b128 v166, v[120:123]\n"
      "v_mfma_f32_32x32x16_bf16 v[48:63], a[212:215], a[72:75], v[48:63]\n"
      "s_add_u32 m0, s15, 0x4000\n"
      "v_add_u32 v23, 0x20000, v23\n"
      "buffer_load_dwordx4 v23, s[48:51], 0 offen offset:0 lds\n"
      "s_waitcnt vmcnt(14) lgkmcnt(4)\n"
      "v_mfma_f32_32x32x16_bf16 v[32:47], a[216:219], a[76:79], v[32:47]\n"
      "ds_read_b64_tr_b16 a[200:201], v10 offset:1024\n"
      "ds_read_b64_tr_b16 a[202:203], v10 offset:5120\n"
      "ds_read_b64_tr_b16 a[204:205], v13 offset:1024\n"
      "ds_read_b64_tr_b16 a[206:207], v13 offset:5120\n"
      "ds_write_b128 v167, v[124:127]\n"
      "v_mfma_f32_32x32x16_bf16 v[48:63], a[220:223], a[76:79], v[48:63]\n"
      "buffer_load_dwordx4 v23, s[48:51], s16 offen offset:1024 lds\n"
      "s_waitcnt vmcnt(14)\n"
      "v_mfma_f32_32x32x16_bf16 v[32:47], a[192:195], a[80:83], v[32:47]\n"
      "ds_read_b64_tr_b16 a[208:209], v10 offset:2048\n"
      "ds_read_b64_tr_b16 a[210:211], v10 offset:6144\n"
      "ds_read_b64_tr_b16 a[212:213], v13 offset:2048\n"
      "ds_read_b64_tr_b16 a[214:215], v13 offset:6144\n"
      "s_waitcnt lgkmcnt(10)\n"
      "v_mfma_f32_32x32x16_bf16 v[48:63], a[196:199], a[80:83], v[48:63]\n"
      "buffer_load_dwordx4 v23, s[48:51], s17 offen offset:2048 lds\n"
      "s_waitcnt vmcnt(14) lgkmcnt(5)\n"
      "v_mfma_f32_32x32x16_bf16 v[32:47], a[200:203], a[84:87], v[32:47]\n"
      "ds_read_b64_tr_b16 a[216:217], v10 offset:3072\n"
      "ds_read_b64_tr_b16 a[218:219], v10 offset:7168\n"
      "ds_read_b64_tr_b16 a[220:221], v13 offset:3072\n"
      "ds_read_b64_tr_b16 a[222:223], v13 offset:7168\n"
      "v_mfma_f32_32x32x16_bf16 v[48:63], a[204:207], a[84:87], v[48:63]\n"
      "buffer_load_dwordx4 v23, s[48:51], s18 offen offset:3072 lds\n"
      "s_waitcnt vmcnt(14) lgkmcnt(4)\n"
      "v_mfma_f32_32x32x16_bf16 v[32:47], a[208:211], a[88:91], v[32:47]\n"
      "v_mfma_f32_32x32x16_bf16 v[48:63], a[212:215], a[88:91], v[48:63]\n"
      "s_waitcnt vmcnt(12) lgkmcnt(0)\n"
      "s_barrier\n"
      "ds_read_b64_tr_b16 a[192:193], v10 offset:16384\n"
      "ds_read_b64_tr_b16 a[194:195], v10 offset:20480\n"
      "s_add_u32 m0, s15, 0x0\n"
      "v_add_u32 v23, 0x20000, v23\n"
      "buffer_load_dwordx4 v23, s[48:51], 0 offen offset:0 lds\n"
      "s_waitcnt vmcnt(12)\n"
      "v_mfma_f32_32x32x16_bf16 v[32:47], a[216:219], a[92:95], v[32:47]\n"
      "ds_read_b64_tr_b16 a[196:197], v13 offset:16384\n"
      "ds_read_b64_tr_b16 a[198:199], v13 offset:20480\n"
      "ds_read_b64_tr_b16 a[200:201], v10 offset:17408\n"
      "ds_read_b64_tr_b16 a[202:203], v10 offset:21504\n"
      "v_mfma_f32_32x32x16_bf16 v[48:63], a[220:223], a[92:95], v[48:63]\n"
      "buffer_load_dwordx4 v23, s[48:51], s16 offen offset:1024 lds\n"
      "s_waitcnt vmcnt(12) lgkmcnt(4)\n"
      "v_mfma_f32_32x32x16_bf16 v[32:47], a[192:195], a[96:99], v[32:47]\n"
      "ds_read_b64_tr_b16 a[204:205], v13 offset:17408\n"
      "ds_read_b64_tr_b16 a[206:207], v13 offset:21504\n"
      "ds_read_b64_tr_b16 a[208:209], v10 offset:18432\n"
      "ds_read_b64_tr_b16 a[210:211], v10 offset:22528\n"
      "ds_read_b128 v[64:67], v168 offset:0\n"
      "s_waitcnt lgkmcnt(7)\n"
      "v_mfma_f32_32x32x16_bf16 v[48:63], a[196:199], a[96:99], v[48:63]\n"
      "buffer_load_dwordx4 v23, s[48:51], s17 offen offset:2048 lds\n"
      "s_waitcnt vmcnt(12) lgkmcnt(5)\n"
      "v_mfma_f32_32x32x16_bf16 v[32:47], a[200:203], a[100:103], v[32:47]\n"
      "ds_read_b64_tr_b16 a[212:213], v13 offset:18432\n"
      "ds_read_b64_tr_b16 a[214:215], v13 offset:22528\n"
      "ds_read_b64_tr_b16 a[216:217], v10 offset:19456\n"
      "ds_read_b64_tr_b16 a[218:219], v10 offset:23552\n"
      "ds_read_b128 v[128:131], v170 offset:0\n"
      "s_waitcnt lgkmcnt(8)\n"
      "v_mfma_f32_32x32x16_bf16 v[48:63], a[204:207], a[100:103], v[48:63]\n"
      "buffer_load_dwordx4 v23, s[48:51], s18 offen offset:3072 lds\n"
      "s_waitcnt vmcnt(12) lgkmcnt(6)\n"
      "v_mfma_f32_32x32x16_bf16 v[32:47], a[208:211], a[104:107], v[32:47]\n"
      "ds_read_b64_tr_b16 a[220:221], v13 offset:19456\n"
      "ds_read_b64_tr_b16 a[222:223], v13 offset:23552\n"
      "ds_read_b64_tr_b16 a[192:193], v10 offset:49152\n"
      "ds_read_b64_tr_b16 a[194:195], v10 offset:53248\n"
      "ds_read_b128 v[68:71], v169 offset:0\n"
      "s_waitcnt lgkmcnt(8)\n"
      "v_mfma_f32_32x32x16_bf16 v[48:63], a[212:215], a[104:107], v[48:63]\n"
      "buffer_load_dwordx4 a[124:127], v174, s[56:59], s78 offen offset:3072 sc1\n"
      "s_waitcnt vmcnt(12) lgkmcnt(6)\n"
      "v_mfma_f32_32x32x16_bf16 v[32:47], a[216:219], a[108:111], v[32:47]\n"
      "ds_read_b64_tr_b16 a[196:197], v13 offset:49152\n"
      "ds_read_b64_tr_b16 a[198:199], v13 offset:53248\n"
      "ds_read_b64_tr_b16 a[200:201], v10 offset:50176\n"
      "ds_read_b64_tr_b16 a[202:203], v10 offset:54272\n"
      "ds_read_b128 v[132:135], v171 offset:0\n"
      "s_waitcnt lgkmcnt(8)\n"
      "v_mfma_f32_32x32x16_bf16 v[48:63], a[220:223], a[108:111], v[48:63]\n"
      "s_add_u32 s78, s75, 0xa0b000\n"
      "buffer_load_dwordx4 a[128:131], v174, s[56:59], s78 offen offset:0 sc1\n"
      "ds_read_b128 v[72:75], v168 offset:2048\n"
      "ds_read_b128 v[136:139], v170 offset:2048\n"
      "ds_read_b128 v[76:79], v169 offset:2048\n"
      "ds_read_b128 v[140:143], v171 offset:2048\n"
      "s_waitcnt vmcnt(12) lgkmcnt(10)\n"
      "v_mfma_f32_32x32x16_bf16 v[32:47], a[192:195], a[112:115], v[32:47]\n"
      "ds_read_b64_tr_b16 a[204:205], v13 offset:50176\n"
      "ds_read_b64_tr_b16 a[206:207], v13 offset:54272\n"
      "ds_read_b64_tr_b16 a[208:209], v10 offset:51200\n"
      "ds_read_b64_tr_b16 a[210:211], v10 offset:55296\n"
      "s_waitcnt lgkmcnt(11)\n"
      "v_mfma_f32_32x32x16_bf16 v[48:63], a[196:199], a[112:115], v[48:63]\n"
      "buffer_load_dwordx4 a[132:135], v174, s[56:59], s78 offen offset:1024 sc1\n"
      "s_waitcnt vmcnt(12) lgkmcnt(9)\n"
      "v_mfma_f32_32x32x16_bf16 v[32:47], a[200:203], a[116:119], v[32:47]\n"
      "ds_read_b64_tr_b16 a[212:213], v13 offset:51200\n"
      "ds_read_b64_tr_b16 a[214:215], v13 offset:55296\n"
      "ds_read_b64_tr_b16 a[216:217], v10 offset:52224\n"
      "ds_read_b64_tr_b16 a[218:219], v10 offset:56320\n"
      "s_waitcnt lgkmcnt(6)\n"
      "v_mfma_f32_32x32x16_bf16 v[48:63], a[204:207], a[116:119], v[48:63]\n"
      "buffer_load_dwordx4 a[136:139], v174, s[56:59], s78 offen offset:2048 sc1\n"
      "s_waitcnt vmcnt(12) lgkmcnt(4)\n"
      "v_mfma_f32_32x32x16_bf16 v[32:47], a[208:211], a[120:123], v[32:47]\n"
      "ds_read_b64_tr_b16 a[220:221], v13 offset:52224\n"
      "ds_read_b64_tr_b16 a[222:223], v13 offset:56320\n"
      "s_waitcnt lgkmcnt(4)\n"
      "v_mfma_f32_32x32x16_bf16 v[48:63], a[212:215], a[120:123], v[48:63]\n"
      "buffer_load_dwordx4 a[140:143], v174, s[56:59], s78 offen offset:3072 sc1\n"
      "s_add_u32 s78, s75, 0xa0d000\n"
      "buffer_load_dwordx4 a[144:147], v174, s[56:59], s78 offen offset:0 sc1\n"
      "buffer_load_dwordx4 a[148:151], v174, s[56:59], s78 offen offset:1024 sc1\n"
      "buffer_load_dwordx4 a[152:155], v174, s[56:59], s78 offen offset:2048 sc1\n"
      "buffer_load_dwordx4 a[156:159], v174, s[56:59], s78 offen offset:3072 sc1\n"
      "s_waitcnt vmcnt(9) lgkmcnt(0)\n"
      "s_barrier\n"
      "ds_read_b64_tr_b16 a[192:193], v9 offset:16384\n"
      "ds_read_b64_tr_b16 a[194:195], v9 offset:20480\n"
      "ds_read_b64_tr_b16 a[196:197], v12 offset:16384\n"
      "ds_read_b64_tr_b16 a[198:199], v12 offset:20480\n"
      "s_waitcnt vmcnt(8)\n"
      "v_mfma_f32_32x32x16_bf16 v[32:47], a[216:219], a[124:127], v[32:47]\n"
      "ds_read_b64_tr_b16 a[200:201], v9 offset:17408\n"
      "ds_read_b64_tr_b16 a[202:203], v9 offset:21504\n"
      "ds_read_b64_tr_b16 a[204:205], v12 offset:17408\n"
      "ds_read_b64_tr_b16 a[206:207], v12 offset:21504\n"
      "v_mfma_f32_32x32x16_bf16 v[48:63], a[220:223], a[124:127], v[48:63]\n"
      "ds_read_b64_tr_b16 a[208:209], v9 offset:18432\n"
      "ds_read_b64_tr_b16 a[210:211], v9 offset:22528\n"
      "ds_read_b64_tr_b16 a[212:213], v12 offset:18432\n"
      "ds_read_b64_tr_b16 a[214:215], v12 offset:22528\n"
      "s_waitcnt vmcnt(7) lgkmcnt(10)\n"
      "v_mfma_f32_32x32x16_bf16 v[32:47], a[192:195], a[128:131], v[32:47]\n"
      "v_add_f32 v64, v64, v128\n"
      "v_add_f32 v65, v65, v129\n"
      "v_add_f32 v66, v66, v130\n"
      "v_add_f32 v67, v67, v131\n"
      "v_add_f32 v68, v68, v132\n"
      "s_waitcnt lgkmcnt(8)\n"
      "v_mfma_f32_32x32x16_bf16 v[48:63], a[196:199], a[128:131], v[48:63]\n"
      "ds_read_b64_tr_b16 a[216:217], v9 offset:19456\n"
      "ds_read_b64_tr_b16 a[218:219], v9 offset:23552\n"
      "v_add_f32 v69, v69, v133\n"
      "ds_read_b64_tr_b16 a[220:221], v12 offset:19456\n"
      "ds_read_b64_tr_b16 a[222:223], v12 offset:23552\n"
      "s_waitcnt vmcnt(6) lgkmcnt(8)\n"
      "v_mfma_f32_32x32x16_bf16 v[32:47], a[200:203], a[132:135], v[32:47]\n"
      "v_add_f32 v70, v70, v134\n"
      "ds_read_b64_tr_b16 a[192:193], v9 offset:0\n"
      "ds_read_b64_tr_b16 a[194:195], v9 offset:4096\n"
      "v_mfma_f32_32x32x16_bf16 v[48:63], a[204:207], a[132:135], v[48:63]\n"
      "v_add_f32 v71, v71, v135\n"
      "ds_read_b64_tr_b16 a[196:197], v12 offset:0\n"
      "ds_read_b64_tr_b16 a[198:199], v12 offset:4096\n"
      "s_waitcnt vmcnt(5) lgkmcnt(8)\n"
      "v_mfma_f32_32x32x16_bf16 v[32:47], a[208:211], a[136:139], v[32:47]\n"
      "v_add_f32 v72, v72, v136\n"
      "ds_read_b64_tr_b16 a[200:201], v9 offset:1024\n"
      "ds_read_b64_tr_b16 a[202:203], v9 offset:5120\n"
      "v_mfma_f32_32x32x16_bf16 v[48:63], a[212:215], a[136:139], v[48:63]\n"
      "v_add_f32 v73, v73, v137\n"
      "ds_read_b64_tr_b16 a[204:205], v12 offset:1024\n"
      "ds_read_b64_tr_b16 a[206:207], v12 offset:5120\n"
      "s_waitcnt vmcnt(4) lgkmcnt(6)\n"
      "v_mfma_f32_32x32x16_bf16 v[32:47], a[216:219], a[140:143], v[32:47]\n"
      "v_add_f32 v74, v74, v138\n"
      "ds_read_b64_tr_b16 a[208:209], v9 offset:2048\n"
      "ds_read_b64_tr_b16 a[210:211], v9 offset:6144\n"
      "v_mfma_f32_32x32x16_bf16 v[48:63], a[220:223], a[140:143], v[48:63]\n"
      "v_add_f32 v75, v75, v139\n"
      "ds_read_b64_tr_b16 a[212:213], v12 offset:2048\n"
      "ds_read_b64_tr_b16 a[214:215], v12 offset:6144\n"
      "s_waitcnt vmcnt(3)\n"
      "v_mfma_f32_32x32x16_bf16 v[32:47], a[192:195], a[144:147], v[32:47]\n"
      "v_add_f32 v76, v76, v140\n"
      "ds_read_b64_tr_b16 a[216:217], v9 offset:3072\n"
      "ds_read_b64_tr_b16 a[218:219], v9 offset:7168\n"
      "s_waitcnt lgkmcnt(6)\n"
      "v_mfma_f32_32x32x16_bf16 v[48:63], a[196:199], a[144:147], v[48:63]\n"
      "v_add_f32 v77, v77, v141\n"
      "ds_read_b64_tr_b16 a[220:221], v12 offset:3072\n"
      "ds_read_b64_tr_b16 a[222:223], v12 offset:7168\n"
      "s_waitcnt vmcnt(2)\n"
      "v_mfma_f32_32x32x16_bf16 v[32:47], a[200:203], a[148:151], v[32:47]\n"
      "v_add_f32 v78, v78, v142\n"
      "v_mfma_f32_32x32x16_bf16 v[48:63], a[204:207], a[148:151], v[48:63]\n"
      "v_add_f32 v79, v79, v143\n"
      "s_waitcnt vmcnt(1) lgkmcnt(4)\n"
      "v_mfma_f32_32x32x16_bf16 v[32:47], a[208:211], a[152:155], v[32:47]\n"
      "v_mfma_f32_32x32x16_bf16 v[48:63], a[212:215], a[152:155], v[48:63]\n"
      "s_waitcnt vmcnt(0) lgkmcnt(0)\n"
      "v_mfma_f32_32x32x16_bf16 v[32:47], a[216:219], a[156:159], v[32:47]\n"
      "v_mfma_f32_32x32x16_bf16 v[48:63], a[220:223], a[156:159], v[48:63]\n"
      "s_nop 10\n"
      "ds_write_b128 v160, v[32:35]\n"
      "ds_write_b128 v161, v[36:39]\n"
      "ds_write_b128 v162, v[40:43]\n"
      "ds_write_b128 v163, v[44:47]\n"
      "ds_write_b128 v164, v[48:51]\n"
      "ds_write_b128 v165, v[52:55]\n"
      "ds_write_b128 v166, v[56:59]\n"
      "ds_write_b128 v167, v[60:63]\n"
      "s_waitcnt lgkmcnt(0)\n"
      "s_barrier\n"
      "ds_read_b128 v[96:99], v168 offset:0\n"
      "ds_read_b128 v[112:115], v170 offset:0\n"
      "ds_read_b128 v[100:103], v169 offset:0\n"
      "ds_read_b128 v[116:119], v171 offset:0\n"
      "ds_read_b128 v[104:107], v168 offset:2048\n"
      "ds_read_b128 v[120:123], v170 offset:2048\n"
      "ds_read_b128 v[108:111], v169 offset:2048\n"
      "ds_read_b128 v[124:127], v171 offset:2048\n"
      "s_waitcnt lgkmcnt(6)\n"
      "v_pk_add_f32 v[96:97], v[96:97], v[112:113]\n"
      "v_pk_add_f32 v[98:99], v[98:99], v[114:115]\n"
      "s_waitcnt lgkmcnt(4)\n"
      "v_pk_add_f32 v[100:101], v[100:101], v[116:117]\n"
      "v_pk_add_f32 v[102:103], v[102:103], v[118:119]\n"
      "v_pk_mul_f32 v[112:113], v[96:97], s[66:67]\n"
      "v_pk_mul_f32 v[114:115], v[98:99], s[66:67]\n"
      "v_pk_mul_f32 v[116:117], v[100:101], s[66:67]\n"
      "v_pk_mul_f32 v[118:119], v[102:103], s[66:67]\n"
      "v_pk_mul_f32 v[96:97], v[96:97], v[64:65]\n"
      "v_pk_mul_f32 v[98:99], v[98:99], v[66:67]\n"
      "v_pk_mul_f32 v[100:101], v[100:101], v[68:69]\n"
      "v_pk_mul_f32 v[102:103], v[102:103], v[70:71]\n"
      "v_exp_f32_e64 v112, -v112\n"
      "v_exp_f32_e64 v113, -v113\n"
      "v_exp_f32_e64 v114, -v114\n"
      "v_exp_f32_e64 v115, -v115\n"
      "v_exp_f32_e64 v116, -v116\n"
      "v_exp_f32_e64 v117, -v117\n"
      "v_exp_f32_e64 v118, -v118\n"
      "v_exp_f32_e64 v119, -v119\n"
      "v_pk_add_f32 v[112:113], v[112:113], 1.0 op_sel_hi:[1,0]\n"
      "v_pk_add_f32 v[114:115], v[114:115], 1.0 op_sel_hi:[1,0]\n"
      "v_pk_add_f32 v[116:117], v[116:117], 1.0 op_sel_hi:[1,0]\n"
      "v_pk_add_f32 v[118:119], v[118:119], 1.0 op_sel_hi:[1,0]\n"
      "v_rcp_f32 v112, v112\n"
      "v_rcp_f32 v113, v113\n"
      "v_rcp_f32 v114, v114\n"
      "v_rcp_f32 v115, v115\n"
      "v_rcp_f32 v116, v116\n"
      "v_rcp_f32 v117, v117\n"
      "v_rcp_f32 v118, v118\n"
      "v_rcp_f32 v119, v119\n"
      "v_pk_fma_f32 v[112:113], v[112:113], v[96:97], v[224:225]\n"
      "v_pk_fma_f32 v[114:115], v[114:115], v[98:99], v[226:227]\n"
      "v_pk_fma_f32 v[116:117], v[116:117], v[100:101], v[228:229]\n"
      "v_pk_fma_f32 v[118:119], v[118:119], v[102:103], v[230:231]\n"
      "v_cvt_pk_bf16_f32 v80, v112, v113\n"
      "v_cvt_pk_bf16_f32 v81, v114, v115\n"
      "v_cvt_pk_bf16_f32 v82, v116, v117\n"
      "v_cvt_pk_bf16_f32 v83, v118, v119\n"
      "buffer_store_dwordx4 v[80:83], v5, s[52:55], s72 offen sc1\n"
      "s_waitcnt lgkmcnt(2)\n"
      "v_pk_add_f32 v[104:105], v[104:105], v[120:121]\n"
      "v_pk_add_f32 v[106:107], v[106:107], v[122:123]\n"
      "s_waitcnt lgkmcnt(0)\n"
      "v_pk_add_f32 v[108:109], v[108:109], v[124:125]\n"
      "v_pk_add_f32 v[110:111], v[110:111], v[126:127]\n"
      "v_pk_mul_f32 v[120:121], v[104:105], s[66:67]\n"
      "v_pk_mul_f32 v[122:123], v[106:107], s[66:67]\n"
      "v_pk_mul_f32 v[124:125], v[108:109], s[66:67]\n"
      "v_pk_mul_f32 v[126:127], v[110:111], s[66:67]\n"
      "v_pk_mul_f32 v[104:105], v[104:105], v[72:73]\n"
      "v_pk_mul_f32 v[106:107], v[106:107], v[74:75]\n"
      "v_pk_mul_f32 v[108:109], v[108:109], v[76:77]\n"
      "v_pk_mul_f32 v[110:111], v[110:111], v[78:79]\n"
      "v_exp_f32_e64 v120, -v120\n"
      "v_exp_f32_e64 v121, -v121\n"
      "v_exp_f32_e64 v122, -v122\n"
      "v_exp_f32_e64 v123, -v123\n"
      "v_exp_f32_e64 v124, -v124\n"
      "v_exp_f32_e64 v125, -v125\n"
      "v_exp_f32_e64 v126, -v126\n"
      "v_exp_f32_e64 v127, -v127\n"
      "v_pk_add_f32 v[120:121], v[120:121], 1.0 op_sel_hi:[1,0]\n"
      "v_pk_add_f32 v[122:123], v[122:123], 1.0 op_sel_hi:[1,0]\n"
      "v_pk_add_f32 v[124:125], v[124:125], 1.0 op_sel_hi:[1,0]\n"
      "v_pk_add_f32 v[126:127], v[126:127], 1.0 op_sel_hi:[1,0]\n"
      "v_rcp_f32 v120, v120\n"
      "v_rcp_f32 v121, v121\n"
      "v_rcp_f32 v122, v122\n"
      "v_rcp_f32 v123, v123\n"
      "v_rcp_f32 v124, v124\n"
      "v_rcp_f32 v125, v125\n"
      "v_rcp_f32 v126, v126\n"
      "v_rcp_f32 v127, v127\n"
      "v_pk_fma_f32 v[120:121], v[120:121], v[104:105], v[232:233]\n"
      "v_pk_fma_f32 v[122:123], v[122:123], v[106:107], v[234:235]\n"
      "v_pk_fma_f32 v[124:125], v[124:125], v[108:109], v[236:237]\n"
      "v_pk_fma_f32 v[126:127], v[126:127], v[110:111], v[238:239]\n"
      "v_cvt_pk_bf16_f32 v84, v120, v121\n"
      "v_cvt_pk_bf16_f32 v85, v122, v123\n"
      "v_cvt_pk_bf16_f32 v86, v124, v125\n"
      "v_cvt_pk_bf16_f32 v87, v126, v127\n"
      "buffer_store_dwordx4 v[84:87], v5, s[52:55], s85 offen sc1\n"
      "s_cmp_lg_u32 s4, 0\n"
      "s_cbranch_scc1 L_end\n"
      "s_mov_b64 exec, 1\n"
      "buffer_store_dwordx2 v[26:27], v1, s[56:59], 0 offen sc1\n"
      "s_endpgm\n"
      "L_spare:\n"
      "s_nop 15\n"
      "s_lshl_b32 s22, s5, 15\n"
      "s_lshr_b32 s78, s9, 1\n"
      "s_lshl_b32 s78, s78, 7\n"
      "s_add_u32 s22, s22, s78\n"
      "s_add_u32 s22, s22, 0x80000\n"
      "v_lshl_add_u32 v22, v0, 11, s22\n"
      "s_add_u32 s3, s63, 0xa0000\n"
      "s_mov_b64 exec, 0xffff\n"
      "buffer_load_dwordx2 v[24:25], v22, s[56:59], 0 offen sc1\n"
      "s_mov_b64 exec, -1\n"
      "v_mul_f32 v224, v202, v128\n"
      "v_mul_f32 v225, v202, v129\n"
      "v_mul_f32 v226, v202, v130\n"
      "v_mul_f32 v227, v202, v131\n"
      "v_fma_f32 v128, v128, v185, s68\n"
      "v_fma_f32 v129, v129, v185, s68\n"
      "v_fma_f32 v130, v130, v185, s68\n"
      "v_fma_f32 v131, v131, v185, s68\n"
      "v_exp_f32_e64 v224, v224 clamp\n"
      "v_exp_f32_e64 v225, v225 clamp\n"
      "v_exp_f32_e64 v226, v226 clamp\n"
      "v_exp_f32_e64 v227, v227 clamp\n"
      "v_max_f32 v128, s68, v128\n"
      "v_max_f32 v129, s68, v129\n"
      "v_max_f32 v130, s68, v130\n"
      "v_max_f32 v131, s68, v131\n"
      "v_add_f32 v128, v128, v224\n"
      "v_add_f32 v129, v129, v225\n"
      "v_add_f32 v130, v130, v226\n"
      "v_add_f32 v131, v131, v227\n"
      "v_cvt_pk_bf16_f32 v88, v128, v129\n"
      "v_cvt_pk_bf16_f32 v89, v130, v131\n"
      "v_mul_f32 v224, v202, v132\n"
      "v_mul_f32 v225, v202, v133\n"
      "v_mul_f32 v226, v202, v134\n"
      "v_mul_f32 v227, v202, v135\n"
      "v_fma_f32 v132, v132, v185, s68\n"
      "v_fma_f32 v133, v133, v185, s68\n"
      "v_fma_f32 v134, v134, v185, s68\n"
      "v_fma_f32 v135, v135, v185, s68\n"
      "v_exp_f32_e64 v224, v224 clamp\n"
      "v_exp_f32_e64 v225, v225 clamp\n"
      "v_exp_f32_e64 v226, v226 clamp\n"
      "v_exp_f32_e64 v227, v227 clamp\n"
      "v_max_f32 v132, s68, v132\n"
      "v_max_f32 v133, s68, v133\n"
      "v_max_f32 v134, s68, v134\n"
      "v_max_f32 v135, s68, v135\n"
      "v_add_f32 v132, v132, v224\n"
      "v_add_f32 v133, v133, v225\n"
      "v_add_f32 v134, v134, v226\n"
      "v_add_f32 v135, v135, v227\n"
      "v_cvt_pk_bf16_f32 v90, v132, v133\n"
      "v_cvt_pk_bf16_f32 v91, v134, v135\n"
      "v_mul_f32 v224, v202, v136\n"
      "v_mul_f32 v225, v202, v137\n"
      "v_mul_f32 v226, v202, v138\n"
      "v_mul_f32 v227, v202, v139\n"
      "v_fma_f32 v136, v136, v185, s68\n"
      "v_fma_f32 v137, v137, v185, s68\n"
      "v_fma_f32 v138, v138, v185, s68\n"
      "v_fma_f32 v139, v139, v185, s68\n"
      "v_exp_f32_e64 v224, v224 clamp\n"
      "v_exp_f32_e64 v225, v225 clamp\n"
      "v_exp_f32_e64 v226, v226 clamp\n"
      "v_exp_f32_e64 v227, v227 clamp\n"
      "v_max_f32 v136, s68, v136\n"
      "v_max_f32 v137, s68, v137\n"
      "v_max_f32 v138, s68, v138\n"
      "v_max_f32 v139, s68, v139\n"
      "v_add_f32 v136, v136, v224\n"
      "v_add_f32 v137, v137, v225\n"
      "v_add_f32 v138, v138, v226\n"
      "v_add_f32 v139, v139, v227\n"
      "v_cvt_pk_bf16_f32 v92, v136, v137\n"
      "v_cvt_pk_bf16_f32 v93, v138, v139\n"
      "v_mul_f32 v224, v202, v140\n"
      "v_mul_f32 v225, v202, v141\n"
      "v_mul_f32 v226, v202, v142\n"
      "v_mul_f32 v227, v202, v143\n"
      "v_fma_f32 v140, v140, v185, s68\n"
      "v_fma_f32 v141, v141, v185, s68\n"
      "v_fma_f32 v142, v142, v185, s68\n"
      "v_fma_f32 v143, v143, v185, s68\n"
      "v_exp_f32_e64 v224, v224 clamp\n"
      "v_exp_f32_e64 v225, v225 clamp\n"
      "v_exp_f32_e64 v226, v226 clamp\n"
      "v_exp_f32_e64 v227, v227 clamp\n"
      "v_max_f32 v140, s68, v140\n"
      "v_max_f32 v141, s68, v141\n"
      "v_max_f32 v142, s68, v142\n"
      "v_max_f32 v143, s68, v143\n"
      "v_add_f32 v140, v140, v224\n"
      "v_add_f32 v141, v141, v225\n"
      "v_add_f32 v142, v142, v226\n"
      "v_add_f32 v143, v143, v227\n"
      "v_cvt_pk_bf16_f32 v94, v140, v141\n"
      "v_cvt_pk_bf16_f32 v95, v142, v143\n"
      "s_waitcnt vmcnt(0) lgkmcnt(0)\n"
      "s_mov_b64 exec, 0xffff\n"
      "v_cmp_eq_u64 vcc, s[60:61], v[24:25]\n"
      "s_nop 0\n"
      "s_cmp_eq_u64 vcc, exec\n"
      "s_cbranch_scc0 L_slow_r\n"
      "L_ok_r:\n"
      "s_mov_b64 exec, -1\n"
      "s_lshl_b32 s22, s9, 2\n"
      "s_add_u32 s22, s22, s5\n"
      "s_lshl_b32 s23, s22, 14\n"
      "s_add_u32 s78, s23, 0x200000\n"
      "buffer_load_dwordx4 v[96:99], v1, s[56:59], s78 offen offset:0 sc1\n"
      "buffer_load_dwordx4 v[100:103], v1, s[56:59], s78 offen offset:1024 sc1\n"
      "buffer_load_dwordx4 v[104:107], v1, s[56:59], s78 offen offset:2048 sc1\n"
      "buffer_load_dwordx4 v[108:111], v1, s[56:59], s78 offen offset:3072 sc1\n"
      "s_add_u32 s78, s23, 0x201000\n"
      "buffer_load_dwordx4 v[112:115], v1, s[56:59], s78 offen offset:0 sc1\n"
      "buffer_load_dwordx4 v[116:119], v1, s[56:59], s78 offen offset:1024 sc1\n"
      "buffer_load_dwordx4 v[120:123], v1, s[56:59], s78 offen offset:2048 sc1\n"
      "buffer_load_dwordx4 v[124:127], v1, s[56:59], s78 offen offset:3072 sc1\n"
      "s_add_u32 s78, s23, 0x202000\n"
      "buffer_load_dwordx4 v[128:131], v1, s[56:59], s78 offen offset:0 sc1\n"
      "buffer_load_dwordx4 v[132:135], v1, s[56:59], s78 offen offset:1024 sc1\n"
      "buffer_load_dwordx4 v[136:139], v1, s[56:59], s78 offen offset:2048 sc1\n"
      "buffer_load_dwordx4 v[140:143], v1, s[56:59], s78 offen offset:3072 sc1\n"
      "s_add_u32 s78, s23, 0x203000\n"
      "buffer_load_dwordx4 v[144:147], v1, s[56:59], s78 offen offset:0 sc1\n"
      "buffer_load_dwordx4 v[148:151], v1, s[56:59], s78 offen offset:1024 sc1\n"
      "buffer_load_dwordx4 v[152:155], v1, s[56:59], s78 offen offset:2048 sc1\n"
      "buffer_load_dwordx4 v[156:159], v1, s[56:59], s78 offen offset:3072 sc1\n"
      "s_lshl_b32 s23, s22, 11\n"
      "s_add_u32 s78, s23, 0x100000\n"
      "buffer_load_dwordx4 v[204:207], v1, s[56:59], s78 offen offset:0 sc1\n"
      "buffer_load_dwordx4 v[208:211], v1, s[56:59], s78 offen offset:1024 sc1\n"
      "s_mul_i32 s21, s93, 14336\n"
      "s_add_u32 s21, s21, s95\n"
      "s_mov_b64 exec, 0x3fff\n"
      "buffer_load_dword a249, v173, s[48:51], s21 offen\n"
      "s_mov_b64 exec, -1\n"
      "s_mul_i32 s21, s93, 4096\n"
      "s_add_u32 s21, s21, s95\n"
      "s_mov_b64 exec, 0xf\n"
      "buffer_load_dword a250, v173, s[32:35], s21 offen\n"
      "s_mov_b64 exec, -1\n"
      "s_barrier\n"
      "s_waitcnt vmcnt(11)\n"
      "v_pk_add_f32 v[96:97], v[96:97], v[128:129]\n"
      "v_pk_add_f32 v[98:99], v[98:99], v[130:131]\n"
      "s_waitcnt vmcnt(10)\n"
      "v_pk_add_f32 v[100:101], v[100:101], v[132:133]\n"
      "v_pk_add_f32 v[102:103], v[102:103], v[134:135]\n"
      "s_waitcnt vmcnt(9)\n"
      "v_pk_add_f32 v[104:105], v[104:105], v[136:137]\n"
      "v_pk_add_f32 v[106:107], v[106:107], v[138:139]\n"
      "s_waitcnt vmcnt(8)\n"
      "v_pk_add_f32 v[108:109], v[108:109], v[140:141]\n"
      "v_pk_add_f32 v[110:111], v[110:111], v[142:143]\n"
      "s_waitcnt vmcnt(7)\n"
      "v_pk_add_f32 v[112:113], v[112:113], v[144:145]\n"
      "v_pk_add_f32 v[114:115], v[114:115], v[146:147]\n"
      "s_waitcnt vmcnt(6)\n"
      "v_pk_add_f32 v[116:117], v[116:117], v[148:149]\n"
      "v_pk_add_f32 v[118:119], v[118:119], v[150:151]\n"
      "s_waitcnt vmcnt(5)\n"
      "v_pk_add_f32 v[120:121], v[120:121], v[152:153]\n"
      "v_pk_add_f32 v[122:123], v[122:123], v[154:155]\n"
      "s_waitcnt vmcnt(4)\n"
      "v_pk_add_f32 v[124:125], v[124:125], v[156:157]\n"
      "v_pk_add_f32 v[126:127], v[126:127], v[158:159]\n"
      "v_pk_add_f32 v[96:97], v[96:97], v[112:113]\n"
      "v_pk_add_f32 v[98:99], v[98:99], v[114:115]\n"
      "v_pk_add_f32 v[100:101], v[100:101], v[116:117]\n"
      "v_pk_add_f32 v[102:103], v[102:103], v[118:119]\n"
      "v_pk_add_f32 v[104:105], v[104:105], v[120:121]\n"
      "v_pk_add_f32 v[106:107], v[106:107], v[122:123]\n"
      "v_pk_add_f32 v[108:109], v[108:109], v[124:125]\n"
      "v_pk_add_f32 v[110:111], v[110:111], v[126:127]\n"
      "v_pk_add_f32 v[96:97], v[96:97], v[104:105]\n"
      "v_pk_add_f32 v[98:99], v[98:99], v[106:107]\n"
      "v_pk_add_f32 v[100:101], v[100:101], v[108:109]\n"
      "v_pk_add_f32 v[102:103], v[102:103], v[110:111]\n"
      "v_pk_add_f32 v[96:97], v[96:97], v[100:101]\n"
      "v_pk_add_f32 v[98:99], v[98:99], v[102:103]\n"
      "s_waitcnt vmcnt(2)\n"
      "v_pk_add_f32 v[204:205], v[204:205], v[208:209]\n"
      "v_pk_add_f32 v[206:207], v[206:207], v[210:211]\n"
      "s_lshr_b32 s22, s15, 2\n"
      "s_add_u32 s22, s22, 0x24000\n"
      "v_add_u32 v224, s22, v1\n"
      "v_add_u32 v225, 0x24000, v1\n"
      "ds_write_b128 v224, v[96:99]\n"
      "ds_write_b128 v224, v[204:207] offset:4096\n"
      "s_waitcnt lgkmcnt(0)\n"
      "s_barrier\n"
      "s_waitcnt vmcnt(0)\n"
      "s_cmp_lg_u32 s5, 0\n"
      "s_cbranch_scc1 L_spare_end\n"
      "ds_read_b128 v[96:99], v225 offset:0\n"
      "ds_read_b128 v[204:207], v225 offset:4096\n"
      "ds_read_b128 v[100:103], v225 offset:1024\n"
      "ds_read_b128 v[208:211], v225 offset:5120\n"
      "ds_read_b128 v[104:107], v225 offset:2048\n"
      "ds_read_b128 v[212:215], v225 offset:6144\n"
      "ds_read_b128 v[108:111], v225 offset:3072\n"
      "ds_read_b128 v[216:219], v225 offset:7168\n"
      "s_waitcnt lgkmcnt(3)\n"
      "v_pk_add_f32 v[96:97], v[96:97], v[104:105]\n"
      "v_pk_add_f32 v[98:99], v[98:99], v[106:107]\n"
      "s_waitcnt lgkmcnt(1)\n"
      "v_pk_add_f32 v[100:101], v[100:101], v[108:109]\n"
      "v_pk_add_f32 v[102:103], v[102:103], v[110:111]\n"
      "v_pk_add_f32 v[96:97], v[96:97], v[100:101]\n"
      "v_pk_add_f32 v[98:99], v[98:99], v[102:103]\n"
      "v_pk_add_f32 v[204:205], v[204:205], v[212:213]\n"
      "v_pk_add_f32 v[206:207], v[206:207], v[214:215]\n"
      "s_waitcnt lgkmcnt(0)\n"
      "v_pk_add_f32 v[208:209], v[208:209], v[216:217]\n"
      "v_pk_add_f32 v[210:211], v[210:211], v[218:219]\n"
      "v_pk_add_f32 v[204:205], v[204:205], v[208:209]\n"
      "v_pk_add_f32 v[206:207], v[206:207], v[210:211]\n"
      "v_cvt_pk_bf16_f32 v96, v96, v97\n"
      "v_cvt_pk_bf16_f32 v97, v98, v99\n"
      "v_lshlrev_b32 v224, 4, v171\n"
      "v_lshl_add_u32 v224, v172, 3, v224\n"
      "s_lshl_b32 s22, s9, 9\n"
      "s_add_u32 s22, s22, 0x4000\n"
      "buffer_store_dwordx2 v[96:97], v224, s[56:59], s22 offen sc1\n"
      "v_add_f32_dpp v204, v204, v204 row_ror:4 row_mask:0xf bank_mask:0xf\n"
      "v_add_f32_dpp v205, v205, v205 row_ror:4 row_mask:0xf bank_mask:0xf\n"
      "v_add_f32_dpp v206, v206, v206 row_ror:4 row_mask:0xf bank_mask:0xf\n"
      "v_add_f32_dpp v207, v207, v207 row_ror:4 row_mask:0xf bank_mask:0xf\n"
      "v_add_f32_dpp v204, v204, v204 row_ror:8 row_mask:0xf bank_mask:0xf\n"
      "v_add_f32_dpp v205, v205, v205 row_ror:8 row_mask:0xf bank_mask:0xf\n"
      "v_add_f32_dpp v206, v206, v206 row_ror:8 row_mask:0xf bank_mask:0xf\n"
      "v_add_f32_dpp v207, v207, v207 row_ror:8 row_mask:0xf bank_mask:0xf\n"
      "v_mov_b32 v208, v204\n"
      "v_mov_b32 v209, v205\n"
      "v_mov_b32 v210, v206\n"
      "v_mov_b32 v211, v207\n"
      "v_permlane16_swap_b32 v204, v208\n"
      "v_permlane16_swap_b32 v205, v209\n"
      "v_permlane16_swap_b32 v206, v210\n"
      "v_permlane16_swap_b32 v207, v211\n"
      "v_add_f32 v204, v204, v208\n"
      "v_add_f32 v205, v205, v209\n"
      "v_add_f32 v206, v206, v210\n"
      "v_add_f32 v207, v207, v211\n"
      "v_mov_b32 v208, v204\n"
      "v_mov_b32 v209, v205\n"
      "v_mov_b32 v210, v206\n"
      "v_mov_b32 v211, v207\n"
      "v_permlane32_swap_b32 v204, v208\n"
      "v_permlane32_swap_b32 v205, v209\n"
      "v_permlane32_swap_b32 v206, v210\n"
      "v_permlane32_swap_b32 v207, v211\n"
      "v_add_f32 v204, v204, v208\n"
      "v_add_f32 v205, v205, v209\n"
      "v_add_f32 v206, v206, v210\n"
      "v_add_f32 v207, v207, v211\n"
      "v_cvt_pk_bf16_f32 v204, v204, v205\n"
      "v_cvt_pk_bf16_f32 v205, v206, v207\n"
      "v_and_b32 v224, 1, v0\n"
      "v_lshlrev_b32 v224, 4, v224\n"
      "v_lshrrev_b32 v225, 1, v0\n"
      "v_lshl_add_u32 v224, v225, 3, v224\n"
      "s_lshl_b32 s78, s9, 7\n"
      "s_add_u32 s22, s78, 0x8000\n"
      "s_mov_b64 exec, 15\n"
      "buffer_store_dwordx2 v[204:205], v224, s[56:59], s22 offen sc1\n"
      "s_mov_b64 exec, -1\n"
      "s_add_u32 s3, s78, 0x2000\n"
      "L_spare_end:\n"
      "s_nop 7\n"
      "s_waitcnt vmcnt(0)\n"
      "s_mov_b64 exec, 1\n"
      "s_xor_b32 s86, s3, 0x0\n"
      "buffer_store_dwordx2 v[26:27], v1, s[56:59], s86 offen sc1\n"
      "s_mov_b64 exec, -1\n"
      "s_add_u32 m0, s15, 0x1c000\n"
      "s_nop 0\n"
      "buffer_load_dwordx4 v23, s[48:51], 0 offen offset:0 lds\n"
      "buffer_load_dwordx4 v23, s[48:51], s16 offen offset:1024 lds\n"
      "buffer_load_dwordx4 v23, s[48:51], s17 offen offset:2048 lds\n"
      "buffer_load_dwordx4 v23, s[48:51], s18 offen offset:3072 lds\n"
      "s_add_u32 m0, s15, 0x4000\n"
      "v_add_u32 v23, 0x20000, v23\n"
      "buffer_load_dwordx4 v23, s[48:51], 0 offen offset:0 lds\n"
      "buffer_load_dwordx4 v23, s[48:51], s16 offen offset:1024 lds\n"
      "buffer_load_dwordx4 v23, s[48:51], s17 offen offset:2048 lds\n"
      "buffer_load_dwordx4 v23, s[48:51], s18 offen offset:3072 lds\n"
      "s_mov_b64 exec, 0x3\n"
      "buffer_load_dwordx2 v[24:25], v201, s[56:59], s81 offen sc1\n"
      "s_mov_b64 exec, -1\n"
      "s_add_u32 m0, s15, 0xc000\n"
      "v_add_u32 v23, 0x20000, v23\n"
      "buffer_load_dwordx4 v23, s[48:51], 0 offen offset:0 lds\n"
      "buffer_load_dwordx4 v23, s[48:51], s16 offen offset:1024 lds\n"
      "buffer_load_dwordx4 v23, s[48:51], s17 offen offset:2048 lds\n"
      "buffer_load_dwordx4 v23, s[48:51], s18 offen offset:3072 lds\n"
      "s_add_u32 m0, s15, 0x20000\n"
      "v_add_u32 v23, 0x20000, v23\n"
      "buffer_load_dwordx4 v23, s[48:51], 0 offen offset:0 lds\n"
      "buffer_load_dwordx4 v23, s[48:51], s16 offen offset:1024 lds\n"
      "buffer_load_dwordx4 v23, s[48:51], s17 offen offset:2048 lds\n"
      "buffer_load_dwordx4 v23, s[48:51], s18 offen offset:3072 lds\n"
      "s_mov_b64 exec, 0x3\n"
      "s_waitcnt vmcnt(8)\n"
      "v_cmp_eq_u64 vcc, s[60:61], v[24:25]\n"
      "s_nop 0\n"
      "s_cmp_eq_u64 vcc, exec\n"
      "s_cbranch_scc0 L_slow_t2\n"
      "L_ok_t2:\n"
      "s_mov_b64 exec, -1\n"
      "buffer_load_dwordx4 a[224:227], v200, s[56:59], 0 offen sc1\n"
      "buffer_load_dwordx4 a[228:231], v203, s[56:59], 0 offen sc1\n"
      "s_add_u32 m0, s15, 0x10000\n"
      "v_add_u32 v23, 0x20000, v23\n"
      "buffer_load_dwordx4 v23, s[48:51], 0 offen offset:0 lds\n"
      "buffer_load_dwordx4 v23, s[48:51], s16 offen offset:1024 lds\n"
      "buffer_load_dwordx4 v23, s[48:51], s17 offen offset:2048 lds\n"
      "buffer_load_dwordx4 v23, s[48:51], s18 offen offset:3072 lds\n"
      "s_add_u32 m0, s15, 0x14000\n"
      "v_add_u32 v23, 0x20000, v23\n"
      "buffer_load_dwordx4 v23, s[48:51], 0 offen offset:0 lds\n"
      "buffer_load_dwordx4 v23, s[48:51], s16 offen offset:1024 lds\n"
      "buffer_load_dwordx4 v23, s[48:51], s17 offen offset:2048 lds\n"
      "buffer_load_dwordx4 v23, s[48:51], s18 offen offset:3072 lds\n"
      "s_add_u32 m0, s15, 0x0\n"
      "v_add_u32 v175, 0xfffe0000, v175\n"
      "buffer_load_dwordx4 v175, s[32:35], 0 offen offset:0 lds\n"
      "buffer_load_dwordx4 v175, s[32:35], s16 offen offset:1024 lds\n"
      "s_waitcnt vmcnt(12)\n"
      "s_barrier\n"
      "ds_read_b64_tr_b16 a[128:129], v10 offset:49152\n"
      "ds_read_b64_tr_b16 a[130:131], v10 offset:53248\n"
      "ds_read_b64_tr_b16 a[132:133], v13 offset:49152\n"
      "ds_read_b64_tr_b16 a[134:135], v13 offset:53248\n"
      "ds_read_b64_tr_b16 a[136:137], v10 offset:50176\n"
      "ds_read_b64_tr_b16 a[138:139], v10 offset:54272\n"
      "ds_read_b64_tr_b16 a[140:141], v13 offset:50176\n"
      "ds_read_b64_tr_b16 a[142:143], v13 offset:54272\n"
      "ds_read_b64_tr_b16 a[144:145], v10 offset:51200\n"
      "ds_read_b64_tr_b16 a[146:147], v10 offset:55296\n"
      "ds_read_b64_tr_b16 a[148:149], v13 offset:51200\n"
      "ds_read_b64_tr_b16 a[150:151], v13 offset:55296\n"
      "ds_read_b64_tr_b16 a[152:153], v10 offset:52224\n"
      "ds_read_b64_tr_b16 a[154:155], v10 offset:56320\n"
      "ds_read_b64_tr_b16 a[156:157], v13 offset:52224\n"
      "ds_read_b64_tr_b16 a[158:159], v13 offset:56320\n"
      "ds_read_b64_tr_b16 a[160:161], v9 offset:16384\n"
      "ds_read_b64_tr_b16 a[162:163], v9 offset:20480\n"
      "ds_read_b64_tr_b16 a[164:165], v12 offset:16384\n"
      "ds_read_b64_tr_b16 a[166:167], v12 offset:20480\n"
      "ds_read_b64_tr_b16 a[168:169], v9 offset:17408\n"
      "ds_read_b64_tr_b16 a[170:171], v9 offset:21504\n"
      "ds_read_b64_tr_b16 a[172:173], v12 offset:17408\n"
      "ds_read_b64_tr_b16 a[174:175], v12 offset:21504\n"
      "ds_read_b64_tr_b16 a[176:177], v9 offset:18432\n"
      "ds_read_b64_tr_b16 a[178:179], v9 offset:22528\n"
      "ds_read_b64_tr_b16 a[180:181], v12 offset:18432\n"
      "ds_read_b64_tr_b16 a[182:183], v12 offset:22528\n"
      "ds_read_b64_tr_b16 a[184:185], v9 offset:19456\n"
      "ds_read_b64_tr_b16 a[186:187], v9 offset:23552\n"
      "ds_read_b64_tr_b16 a[188:189], v12 offset:19456\n"
      "ds_read_b64_tr_b16 a[190:191], v12 offset:23552\n"
      "ds_read_b64_tr_b16 a[0:1], v9 offset:49152\n"
      "ds_read_b64_tr_b16 a[2:3], v9 offset:53248\n"
      "ds_read_b64_tr_b16 a[4:5], v12 offset:49152\n"
      "ds_read_b64_tr_b16 a[6:7], v12 offset:53248\n"
      "ds_read_b64_tr_b16 a[8:9], v9 offset:50176\n"
      "ds_read_b64_tr_b16 a[10:11], v9 offset:54272\n"
      "ds_read_b64_tr_b16 a[12:13], v12 offset:50176\n"
      "ds_read_b64_tr_b16 a[14:15], v12 offset:54272\n"
      "ds_read_b64_tr_b16 a[16:17], v9 offset:51200\n"
      "ds_read_b64_tr_b16 a[18:19], v9 offset:55296\n"
      "ds_read_b64_tr_b16 a[20:21], v12 offset:51200\n"
      "ds_read_b64_tr_b16 a[22:23], v12 offset:55296\n"
      "ds_read_b64_tr_b16 a[24:25], v9 offset:52224\n"
      "ds_read_b64_tr_b16 a[26:27], v9 offset:56320\n"
      "ds_read_b64_tr_b16 a[28:29], v12 offset:52224\n"
      "ds_read_b64_tr_b16 a[30:31], v12 offset:56320\n"
      "ds_read_b64_tr_b16 a[32:33], v11 offset:0\n"
      "ds_read_b64_tr_b16 a[34:35], v11 offset:4096\n"
      "ds_read_b64_tr_b16 a[36:37], v14 offset:0\n"
      "ds_read_b64_tr_b16 a[38:39], v14 offset:4096\n"
      "ds_read_b64_tr_b16 a[40:41], v11 offset:1024\n"
      "ds_read_b64_tr_b16 a[42:43], v11 offset:5120\n"
      "ds_read_b64_tr_b16 a[44:45], v14 offset:1024\n"
      "ds_read_b64_tr_b16 a[46:47], v14 offset:5120\n"
      "ds_read_b64_tr_b16 a[48:49], v11 offset:2048\n"
      "ds_read_b64_tr_b16 a[50:51], v11 offset:6144\n"
      "ds_read_b64_tr_b16 a[52:53], v14 offset:2048\n"
      "ds_read_b64_tr_b16 a[54:55], v14 offset:6144\n"
      "ds_read_b64_tr_b16 a[56:57], v11 offset:3072\n"
      "ds_read_b64_tr_b16 a[58:59], v11 offset:7168\n"
      "ds_read_b64_tr_b16 a[60:61], v14 offset:3072\n"
      "ds_read_b64_tr_b16 a[62:63], v14 offset:7168\n"
      "s_waitcnt lgkmcnt(0)\n"
      "s_barrier\n"
      "buffer_load_dwordx4 v175, s[32:35], s17 offen offset:2048 lds\n"
      "buffer_load_dwordx4 v175, s[32:35], s18 offen offset:3072 lds\n"
      "s_add_u32 m0, s15, 0x8000\n"
      "v_add_u32 v175, 0x10000, v175\n"
      "buffer_load_dwordx4 v175, s[32:35], 0 offen offset:0 lds\n"
      "s_waitcnt vmcnt(14)\n"
      "v_mfma_f32_32x32x16_bf16 v[128:143], a[224:227], v[88:91], 0\n"
      "s_waitcnt vmcnt(13)\n"
      "v_mfma_f32_32x32x16_bf16 v[144:159], a[228:231], v[92:95], 0\n"
      "s_add_u32 s22, s74, 0x800000\n"
      "s_nop 8\n"
      "v_add_f32 v136, s68, v136\n"
      "v_rcp_f32 v136, v136\n"
      "v_add_f32 v152, s68, v152\n"
      "v_rcp_f32 v152, v152\n"
      "v_mul_f32 v128, v128, v136\n"
      "v_mul_f32 v129, v129, v136\n"
      "v_mul_f32 v130, v130, v136\n"
      "v_mul_f32 v131, v131, v136\n"
      "v_mul_f32 v132, v132, v136\n"
      "v_mul_f32 v133, v133, v136\n"
      "v_mul_f32 v134, v134, v136\n"
      "v_mul_f32 v135, v135, v136\n"
      "v_cvt_pk_bf16_f32 v88, v128, v129\n"
      "v_cvt_pk_bf16_f32 v89, v130, v131\n"
      "v_cvt_pk_bf16_f32 v90, v132, v133\n"
      "v_cvt_pk_bf16_f32 v91, v134, v135\n"
      "buffer_store_dwordx4 v[88:91], v174, s[56:59], s22 offen offset:0 sc1\n"
      "v_mul_f32 v144, v144, v152\n"
      "v_mul_f32 v145, v145, v152\n"
      "v_mul_f32 v146, v146, v152\n"
      "v_mul_f32 v147, v147, v152\n"
      "v_mul_f32 v148, v148, v152\n"
      "v_mul_f32 v149, v149, v152\n"
      "v_mul_f32 v150, v150, v152\n"
      "v_mul_f32 v151, v151, v152\n"
      "v_cvt_pk_bf16_f32 v92, v144, v145\n"
      "v_cvt_pk_bf16_f32 v93, v146, v147\n"
      "v_cvt_pk_bf16_f32 v94, v148, v149\n"
      "v_cvt_pk_bf16_f32 v95, v150, v151\n"
      "buffer_store_dwordx4 v[92:95], v174, s[56:59], s22 offen offset:1024 sc1\n"
      "buffer_load_dwordx4 v175, s[32:35], s16 offen offset:1024 lds\n"
      "buffer_load_dwordx4 v175, s[32:35], s17 offen offset:2048 lds\n"
      "buffer_load_dwordx4 v[80:83], v5, s[24:27], s72 offen\n"
      "buffer_load_dwordx4 v[84:87], v5, s[24:27], s85 offen\n"
      "s_add_u32 m0, s15, 0x1c000\n"
      "v_add_u32 v23, 0x20000, v23\n"
      "buffer_load_dwordx4 v23, s[48:51], 0 offen offset:0 lds\n"
      "buffer_load_dwordx4 v23, s[48:51], s16 offen offset:1024 lds\n"
      "buffer_load_dwordx4 v23, s[48:51], s17 offen offset:2048 lds\n"
      "buffer_load_dwordx4 v23, s[48:51], s18 offen offset:3072 lds\n"
      "s_add_u32 m0, s15, 0x4000\n"
      "v_add_u32 v175, 0xfffe0000, v175\n"
      "buffer_load_dwordx4 v175, s[32:35], 0 offen offset:0 lds\n"
      "buffer_load_dwordx4 v175, s[32:35], s16 offen offset:1024 lds\n"
      "s_waitcnt vmcnt(10)\n"
      "s_mov_b64 exec, 1\n"
      "s_xor_b32 s86, s79, 0x60000\n"
      "buffer_store_dwordx2 v[26:27], v1, s[56:59], s86 offen sc1\n"
      "s_mov_b64 exec, -1\n"
      "L_late:\n"
      "buffer_load_dwordx4 v175, s[32:35], s17 offen offset:2048 lds\n"
      "buffer_load_dwordx4 v175, s[32:35], s18 offen offset:3072 lds\n"
      "s_mov_b64 exec, 0x7f\n"
      "buffer_load_dwordx2 v[24:25], v201, s[56:59], s83 offen sc1\n"
      "s_mov_b64 exec, -1\n"
      "s_waitcnt vmcnt(0) lgkmcnt(0)\n"
      "s_barrier\n"
      "s_add_u32 m0, s15, 0x8000\n"
      "v_add_u32 v175, 0x20000, v175\n"
      "buffer_load_dwordx4 v175, s[32:35], s18 offen offset:3072 lds\n"
      "s_add_u32 m0, s15, 0xc000\n"
      "v_add_u32 v175, 0x10000, v175\n"
      "buffer_load_dwordx4 v175, s[32:35], 0 offen offset:0 lds\n"
      "buffer_load_dwordx4 v175, s[32:35], s16 offen offset:1024 lds\n"
      "buffer_load_dwordx4 v175, s[32:35], s17 offen offset:2048 lds\n"
      "buffer_load_dwordx4 v175, s[32:35], s18 offen offset:3072 lds\n"
      "s_add_u32 m0, s15, 0x20000\n"
      "v_add_u32 v23, 0xfff50000, v23\n"
      "buffer_load_dwordx4 v23, s[48:51], 0 offen offset:0 lds\n"
      "buffer_load_dwordx4 v23, s[48:51], s16 offen offset:1024 lds\n"
      "buffer_load_dwordx4 v23, s[48:51], s17 offen offset:2048 lds\n"
      "buffer_load_dwordx4 v23, s[48:51], s18 offen offset:3072 lds\n"
      "s_add_u32 m0, s15, 0x18000\n"
      "v_add_u32 v23, 0x20000, v23\n"
      "buffer_load_dwordx4 v23, s[48:51], 0 offen offset:0 lds\n"
      "buffer_load_dwordx4 v23, s[48:51], s16 offen offset:1024 lds\n"
      "s_mov_b64 exec, 0x7f\n"
      "v_cmp_eq_u64 vcc, s[60:61], v[24:25]\n"
      "s_nop 0\n"
      "s_cmp_eq_u64 vcc, exec\n"
      "s_cbranch_scc0 L_slow_gs\n"
      "L_ok_gs:\n"
      "s_mov_b64 exec, -1\n"
      "s_add_u32 s78, s75, 0xa00000\n"
      "buffer_load_dwordx4 a[64:67], v174, s[56:59], s78 offen offset:0 sc1\n"
      "buffer_load_dwordx4 a[68:71], v174, s[56:59], s78 offen offset:1024 sc1\n"
      "buffer_load_dwordx4 a[72:75], v174, s[56:59], s78 offen offset:2048 sc1\n"
      "buffer_load_dwordx4 a[76:79], v174, s[56:59], s78 offen offset:3072 sc1\n"
      "s_add_u32 s78, s75, 0xa02000\n"
      "buffer_load_dwordx4 a[80:83], v174, s[56:59], s78 offen offset:0 sc1\n"
      "buffer_load_dwordx4 a[84:87], v174, s[56:59], s78 offen offset:1024 sc1\n"
      "buffer_load_dword a253, v1, s[44:47], 0 offen\n"
      "buffer_load_dwordx4 a[88:91], v174, s[56:59], s78 offen offset:2048 sc1\n"
      "buffer_load_dwordx4 a[92:95], v174, s[56:59], s78 offen offset:3072 sc1\n"
      "s_add_u32 s78, s75, 0xa04000\n"
      "buffer_load_dwordx4 a[96:99], v174, s[56:59], s78 offen offset:0 sc1\n"
      "buffer_load_dwordx4 a[100:103], v174, s[56:59], s78 offen offset:1024 sc1\n"
      "buffer_load_dword a253, v1, s[44:47], 0 offen\n"
      "buffer_load_dwordx4 a[104:107], v174, s[56:59], s78 offen offset:2048 sc1\n"
      "buffer_load_dwordx4 a[108:111], v174, s[56:59], s78 offen offset:3072 sc1\n"
      "s_add_u32 s78, s75, 0xa06000\n"
      "buffer_load_dwordx4 a[112:115], v174, s[56:59], s78 offen offset:0 sc1\n"
      "buffer_load_dwordx4 a[116:119], v174, s[56:59], s78 offen offset:1024 sc1\n"
      "buffer_load_dword a253, v1, s[44:47], 0 offen\n"
      "buffer_load_dwordx4 a[120:123], v174, s[56:59], s78 offen offset:2048 sc1\n"
      "buffer_load_dwordx4 a[124:127], v174, s[56:59], s78 offen offset:3072 sc1\n"
      "buffer_load_dwordx4 v23, s[48:51], s17 offen offset:2048 lds\n"
      "buffer_load_dwordx4 v23, s[48:51], s18 offen offset:3072 lds\n"
      "buffer_load_dword a253, v1, s[44:47], 0 offen\n"
      "s_nop 15\n"
      "s_branch L_merge\n"
      "L_slow_h:\n"
      "s_mov_b32 s62, 0\n"
      "L_loop_h:\n"
      "buffer_load_dwordx2 v[24:25], v22, s[56:59], 0 offen sc1\n"
      "s_waitcnt vmcnt(0)\n"
      "v_cmp_eq_u64 vcc, s[60:61], v[24:25]\n"
      "s_nop 1\n"
      "s_cmp_eq_u64 vcc, exec\n"
      "s_cbranch_scc1 L_ok_h\n"
      "s_sleep 0\n"
      "s_add_u32 s62, s62, 1\n"
      "s_cmp_lt_u32 s62, 0x100000\n"
      "s_cbranch_scc1 L_loop_h\n"
      "s_branch L_ok_h\n"
      "L_slow_x:\n"
      "s_mov_b32 s62, 0\n"
      "L_loop_x:\n"
      "buffer_load_dwordx2 v[24:25], v201, s[56:59], s80 offen sc1\n"
      "s_waitcnt vmcnt(0)\n"
      "v_cmp_eq_u64 vcc, s[60:61], v[24:25]\n"
      "s_nop 1\n"
      "s_cmp_eq_u64 vcc, exec\n"
      "s_cbranch_scc1 L_ok_x\n"
      "s_sleep 0\n"
      "s_add_u32 s62, s62, 1\n"
      "s_cmp_lt_u32 s62, 0x100000\n"
      "s_cbranch_scc1 L_loop_x\n"
      "s_branch L_ok_x\n"
      "L_slow_t:\n"
      "s_mov_b32 s62, 0\n"
      "L_loop_t:\n"
      "buffer_load_dwordx2 v[24:25], v201, s[56:59], s81 offen sc1\n"
      "s_waitcnt vmcnt(0)\n"
      "v_cmp_eq_u64 vcc, s[60:61], v[24:25]\n"
      "s_nop 1\n"
      "s_cmp_eq_u64 vcc, exec\n"
      "s_cbranch_scc1 L_ok_t\n"
      "s_sleep 0\n"
      "s_add_u32 s62, s62, 1\n"
      "s_cmp_lt_u32 s62, 0x100000\n"
      "s_cbranch_scc1 L_loop_t\n"
      "s_branch L_ok_t\n"
      "L_slow_g:\n"
      "s_mov_b32 s62, 0\n"
      "L_loop_g:\n"
      "buffer_load_dwordx2 v[24:25], v201, s[56:59], s83 offen sc1\n"
      "s_waitcnt vmcnt(0)\n"
      "v_cmp_eq_u64 vcc, s[60:61], v[24:25]\n"
      "s_nop 1\n"
      "s_cmp_eq_u64 vcc, exec\n"
      "s_cbranch_scc1 L_ok_g\n"
      "s_sleep 0\n"
      "s_add_u32 s62, s62, 1\n"
      "s_cmp_lt_u32 s62, 0x100000\n"
      "s_cbranch_scc1 L_loop_g\n"
      "s_branch L_ok_g\n"
      "L_slow_o:\n"
      "s_mov_b32 s62, 0\n"
      "L_loop_o:\n"
      "buffer_load_dwordx2 v[24:25], v201, s[56:59], s82 offen sc1\n"
      "s_waitcnt vmcnt(0)\n"
      "v_cmp_eq_u64 vcc, s[60:61], v[24:25]\n"
      "s_nop 1\n"
      "s_cmp_eq_u64 vcc, exec\n"
      "s_cbranch_scc1 L_ok_o\n"
      "s_sleep 0\n"
      "s_add_u32 s62, s62, 1\n"
      "s_cmp_lt_u32 s62, 0x100000\n"
      "s_cbranch_scc1 L_loop_o\n"
      "s_branch L_ok_o\n"
      "L_slow_b:\n"
      "s_mov_b32 s62, 0\n"
      "L_loop_b:\n"
      "buffer_load_dwordx2 v[24:25], v201, s[56:59], s84 offen sc1\n"
      "s_waitcnt vmcnt(0)\n"
      "v_cmp_eq_u64 vcc, s[60:61], v[24:25]\n"
      "s_nop 1\n"
      "s_cmp_eq_u64 vcc, exec\n"
      "s_cbranch_scc1 L_ok_b\n"
      "s_sleep 0\n"
      "s_add_u32 s62, s62, 1\n"
      "s_cmp_lt_u32 s62, 0x100000\n"
      "s_cbranch_scc1 L_loop_b\n"
      "s_branch L_ok_b\n"
      "L_slow_r:\n"
      "s_mov_b32 s62, 0\n"
      "L_loop_r:\n"
      "buffer_load_dwordx2 v[24:25], v22, s[56:59], 0 offen sc1\n"
      "s_waitcnt vmcnt(0)\n"
      "v_cmp_eq_u64 vcc, s[60:61], v[24:25]\n"
      "s_nop 1\n"
      "s_cmp_eq_u64 vcc, exec\n"
      "s_cbranch_scc1 L_ok_r\n"
      "s_sleep 0\n"
      "s_add_u32 s62, s62, 1\n"
      "s_cmp_lt_u32 s62, 0x100000\n"
      "s_cbranch_scc1 L_loop_r\n"
      "s_branch L_ok_r\n"
      "L_slow_t2:\n"
      "s_mov_b32 s62, 0\n"
      "L_loop_t2:\n"
      "buffer_load_dwordx2 v[24:25], v201, s[56:59], s81 offen sc1\n"
      "s_waitcnt vmcnt(0)\n"
      "v_cmp_eq_u64 vcc, s[60:61], v[24:25]\n"
      "s_nop 1\n"
      "s_cmp_eq_u64 vcc, exec\n"
      "s_cbranch_scc1 L_ok_t2\n"
      "s_sleep 0\n"
      "s_add_u32 s62, s62, 1\n"
      "s_cmp_lt_u32 s62, 0x100000\n"
      "s_cbranch_scc1 L_loop_t2\n"
      "s_branch L_ok_t2\n"
      "L_slow_gs:\n"
      "s_mov_b32 s62, 0\n"
      "L_loop_gs:\n"
      "buffer_load_dwordx2 v[24:25], v201, s[56:59], s83 offen sc1\n"
      "s_waitcnt vmcnt(0)\n"
      "v_cmp_eq_u64 vcc, s[60:61], v[24:25]\n"
      "s_nop 1\n"
      "s_cmp_eq_u64 vcc, exec\n"
      "s_cbranch_scc1 L_ok_gs\n"
      "s_sleep 0\n"
      "s_add_u32 s62, s62, 1\n"
      "s_cmp_lt_u32 s62, 0x100000\n"
      "s_cbranch_scc1 L_loop_gs\n"
      "s_branch L_ok_gs\n"
      "L_end:\n"
      "s_endpgm\n"
      :
      : "s"(__builtin_amdgcn_kernarg_segment_ptr()), "s"(blockIdx.x), "v"(threadIdx.x)
      : "memory", "vcc", "s3", "s4", "s5", "s6", "s7", "s8", "s9", "s10", "s11", "s12", "s13", "s14", "s15", "s16", "s17", "s18", "s19", "s20", "s21", "s22", "s23", "s24", "s25", "s26", "s27", "s28", "s29", "s30", "s31", "s33", "s34", "s35", "s36", "s37", "s38", "s39", "s40", "s41", "s42", "s43", "s44", "s45", "s46", "s47", "s48", "s49", "s50", "s51", "s52", "s53", "s54", "s55", "s56", "s57", "s58", "s59", "s60", "s61", "s62", "s63", "s64", "s65", "s66", "s67", "s68", "s69", "s70", "s71", "s72", "s73", "s74", "s75", "s76", "s77", "s78", "s79", "s80", "s81", "s82", "s83", "s84", "s85", "s86", "s87", "s88", "s89", "s90", "s91", "s92", "s93", "s94", "s95", "s96", "s97", "s98", "s99", "v1", "v2", "v3", "v4", "v5", "v6", "v7", "v8", "v9", "v10", "v11", "v12", "v13", "v14", "v15", "v16", "v17", "v18", "v19", "v20", "v21", "v22", "v23", "v24", "v25", "v26", "v27", "v28", "v29", "v30", "v31", "v32", "v33", "v34", "v35", "v36", "v37", "v38", "v39", "v40", "v41", "v42", "v43", "v44", "v45", "v46", "v47", "v48", "v49", "v50", "v51", "v52", "v53", "v54", "v55", "v56", "v57", "v58", "v59", "v60", "v61", "v62", "v63", "v64", "v65", "v66", "v67", "v68", "v69", "v70", "v71", "v72", "v73", "v74", "v75", "v76", "v77", "v78", "v79", "v80", "v81", "v82", "v83", "v84", "v85", "v86", "v87", "v88", "v89", "v90", "v91", "v92", "v93", "v94", "v95", "v96", "v97", "v98", "v99", "v100", "v101", "v102", "v103", "v104", "v105", "v106", "v107", "v108", "v109", "v110", "v111", "v112", "v113", "v114", "v115", "v116", "v117", "v118", "v119", "v120", "v121", "v122", "v123", "v124", "v125", "v126", "v127", "v128", "v129", "v130", "v131", "v132", "v133", "v134", "v135", "v136", "v137", "v138", "v139", "v140", "v141", "v142", "v143", "v144", "v145", "v146", "v147", "v148", "v149", "v150", "v151", "v152", "v153", "v154", "v155", "v156", "v157", "v158", "v159", "v160", "v161", "v162", "v163", "v164", "v165", "v166", "v167", "v168", "v169", "v170", "v171", "v172", "v173", "v174", "v175", "v176", "v177", "v178", "v179", "v180", "v181", "v182", "v183", "v184", "v185", "v186", "v187", "v188", "v189", "v190", "v191", "v192", "v193", "v194", "v195", "v196", "v197", "v198", "v199", "v200", "v201", "v202", "v203", "v204", "v205", "v206", "v207", "v208", "v209", "v210", "v211", "v212", "v213", "v214", "v215", "v216", "v217", "v218", "v219", "v220", "v221", "v222", "v223", "v224", "v225", "v226", "v227", "v228", "v229", "v230", "v231", "v232", "v233", "v234", "v235", "v236", "v237", "v238", "v239", "v240", "v241", "v242", "v243", "v244", "v245", "v246", "v247", "v248", "v249", "v250", "v251", "v252", "v253", "v254", "v255", "a0", "a1", "a2", "a3", "a4", "a5", "a6", "a7", "a8", "a9", "a10", "a11", "a12", "a13", "a14", "a15", "a16", "a17", "a18", "a19", "a20", "a21", "a22", "a23", "a24", "a25", "a26", "a27", "a28", "a29", "a30", "a31", "a32", "a33", "a34", "a35", "a36", "a37", "a38", "a39", "a40", "a41", "a42", "a43", "a44", "a45", "a46", "a47", "a48", "a49", "a50", "a51", "a52", "a53", "a54", "a55", "a56", "a57", "a58", "a59", "a60", "a61", "a62", "a63", "a64", "a65", "a66", "a67", "a68", "a69", "a70", "a71", "a72", "a73", "a74", "a75", "a76", "a77", "a78", "a79", "a80", "a81", "a82", "a83", "a84", "a85", "a86", "a87", "a88", "a89", "a90", "a91", "a92", "a93", "a94", "a95", "a96", "a97", "a98", "a99", "a100", "a101", "a102", "a103", "a104", "a105", "a106", "a107", "a108", "a109", "a110", "a111", "a112", "a113", "a114", "a115", "a116", "a117", "a118", "a119", "a120", "a121", "a122", "a123", "a124", "a125", "a126", "a127", "a128", "a129", "a130", "a131", "a132", "a133", "a134", "a135", "a136", "a137", "a138", "a139", "a140", "a141", "a142", "a143", "a144", "a145", "a146", "a147", "a148", "a149", "a150", "a151", "a152", "a153", "a154", "a155", "a156", "a157", "a158", "a159", "a160", "a161", "a162", "a163", "a164", "a165", "a166", "a167", "a168", "a169", "a170", "a171", "a172", "a173", "a174", "a175", "a176", "a177", "a178", "a179", "a180", "a181", "a182", "a183", "a184", "a185", "a186", "a187", "a188", "a189", "a190", "a191", "a192", "a193", "a194", "a195", "a196", "a197", "a198", "a199", "a200", "a201", "a202", "a203", "a204", "a205", "a206", "a207", "a208", "a209", "a210", "a211", "a212", "a213", "a214", "a215", "a216", "a217", "a218", "a219", "a220", "a221", "a222", "a223", "a224", "a225", "a226", "a227", "a228", "a229", "a230", "a231", "a232", "a233", "a234", "a235", "a236", "a237", "a238", "a239", "a240", "a241", "a242", "a243", "a244", "a245", "a246", "a247", "a248", "a249", "a250", "a251", "a252", "a253", "a254", "a255");
#endif
}

extern "C" void kernel_launch(void* const* d_in, const int* in_sizes, int n_in, void* d_out, int out_size,
                              void* d_ws, size_t ws_size, hipStream_t stream) {
  const unsigned F = 0x00020000u;
  fused_block_kernel<<<256, 256, 0, stream>>>(
      d_in[0], 2048u * 512u * 2u, F, d_in[1], 512u * 1536u * 2u, F, d_in[2], 512u * 512u * 2u, F,
      d_in[4], 512u * 512u * 2u, F, d_in[5], 512u * 512u * 2u, F, d_in[6], 512u * 1792u * 2u, F,
      d_in[7], 1792u * 512u * 2u, F, d_out, 2048u * 512u * 2u, F, d_ws, 41943040u, F);
}
```
